```python
import torch

def _softmax(x, axis=-1):
    return torch.softmax(x, dim=axis)
def _gelu(x):
    return torch.nn.functional.gelu(x, approximate="tanh")
def _silu(x):
    return torch.nn.functional.silu(x)
def _sigmoid(x):
    return torch.sigmoid(x)
def _layernorm(x, axis=-1, eps=1e-5):
    m = x.mean(dim=axis, keepdim=True)
    v = x.var(dim=axis, keepdim=True, unbiased=False)
    return (x - m) / torch.sqrt(v + eps)
def _scatter_add(tgt, idx, src, axis=0):
    return tgt.clone().index_add_(axis, idx, src)
def _segment_sum(src, seg, n):
    out = torch.zeros((n,) + src.shape[1:], dtype=src.dtype)
    return out.index_add_(0, seg, src)
def _pad(x, pw):
    flat = tuple(v for pair in reversed(pw) for v in pair)
    return torch.nn.functional.pad(x, flat)

kernel_name = 'recurrent_scan__ssm_conv_s4d__geglu_ffn__prompt'

def setup_inputs():

    _w = (lambda a: (lambda t: t.to(torch.bfloat16) if t.is_floating_point() else t)(torch.as_tensor(a)))
    def _A_setup():
        Bsz, L, D, N = 1, 2048, 512, 128
        return {
            "u": _w((lambda *s: torch.randn(*s, dtype=torch.bfloat16) * 0.05)(Bsz, L, D)),
            "log_A_real": _w((lambda *s: torch.randn(*s, dtype=torch.bfloat16) * 6.4)(D, N) * 0.5),
            "A_imag": _w((lambda *s: torch.randn(*s, dtype=torch.bfloat16) * 0.05)(D, N) * 3.14),
            "C_real": _w((lambda *s: torch.randn(*s, dtype=torch.bfloat16) * 0.05)(D, N) / N ** 0.5),
            "C_imag": _w((lambda *s: torch.randn(*s, dtype=torch.bfloat16) * 0.05)(D, N) / N ** 0.5),
            "log_dt": _w((lambda *s: torch.randn(*s, dtype=torch.bfloat16) * 0.05)(D) * 0.5 - 4.0),
            "D_skip": _w((lambda *s: torch.randn(*s, dtype=torch.bfloat16) * 0.05)(D)),
        }
    _A_in = _A_setup()

    def _B_setup():
        B, S, D, F = 1, 2048, 512, 1792
        return {
            "x": _w((lambda *s: torch.randn(*s, dtype=torch.bfloat16) * 0.05)(B, S, D)),
            "w_gate": _w((lambda *s: torch.randn(*s, dtype=torch.bfloat16) * 0.05)(D, F)),
            "w_up": _w((lambda *s: torch.randn(*s, dtype=torch.bfloat16) * 0.05)(D, F)),
            "w_down": _w((lambda *s: torch.randn(*s, dtype=torch.bfloat16) * 0.05)(F, D)),
        }
    _B_in = _B_setup()

    return {
        "A_u": _A_in["u"],
        "A_log_A_real": _A_in["log_A_real"],
        "A_A_imag": _A_in["A_imag"],
        "A_C_real": _A_in["C_real"],
        "A_C_imag": _A_in["C_imag"],
        "A_log_dt": _A_in["log_dt"],
        "A_D_skip": _A_in["D_skip"],
        "B_x": _B_in["x"],
        "B_w_gate": _B_in["w_gate"],
        "B_w_up": _B_in["w_up"],
        "B_w_down": _B_in["w_down"],
    }

def reference(A_u, A_log_A_real, A_A_imag, A_C_real, A_C_imag, A_log_dt, A_D_skip, B_x, B_w_gate, B_w_up, B_w_down):
    def _A_ref(u, log_A_real, A_imag, C_real, C_imag, log_dt, D_skip):
        L = u.shape[1]
        dt = torch.exp(log_dt)
        A = -torch.exp(log_A_real) + 1j * A_imag
        C = (C_real + 1j * C_imag) * dt[:, None]
        pos = (lambda x, dt: x.to(dt))(torch.arange(L), log_dt.dtype)
        P = torch.exp((A * dt[:, None])[:, :, None] * pos[None, None, :])
        K = 2.0 * torch.einsum("dn,dnl->dl", C, P).real
        L2 = 2 * L
        u_t = (lambda x, *d: x.permute(*d))(u, 0, 2, 1)
        Kf = (lambda x, *a, **k: torch.fft.rfft(x.float(), *a, **k))(K, L2)
        Uf = (lambda x, *a, **k: torch.fft.rfft(x.float(), *a, **k))(u_t, L2)
        y = (lambda x, *a, **k: torch.fft.irfft(x, *a, **k).to(torch.bfloat16))(Uf * Kf[None, :, :], L2)[:, :, :L]
        y = (lambda x, *d: x.permute(*d))(y, 0, 2, 1)
        return y + u * D_skip[None, None, :]

    def _B_ref(x, w_gate, w_up, w_down):
        return (_gelu(x @ w_gate) * (x @ w_up)) @ w_down

    x = A_u
    xn = _layernorm(x, axis=-1)
    s = _A_ref(xn, A_log_A_real, A_A_imag, A_C_real, A_C_imag, A_log_dt, A_D_skip)
    g = _silu(_B_ref(xn, B_w_gate, B_w_up, B_w_down))
    return x + s * g
```

```cpp
#include <hip/hip_runtime.h>
#include <hip/hip_bf16.h>
#include <stdint.h>

typedef __attribute__((ext_vector_type(8))) __bf16 bf16x8;
typedef __attribute__((ext_vector_type(8))) _Float16 f16x8;
typedef __attribute__((ext_vector_type(2))) _Float16 f16x2;
typedef __attribute__((ext_vector_type(4))) float f32x4;
typedef __attribute__((ext_vector_type(4))) unsigned int u32x4;
typedef __attribute__((ext_vector_type(2))) unsigned int u32x2;
typedef unsigned short u16;

constexpr int L_ = 2048, D_ = 512, NS_ = 128, F_ = 1792;
constexpr int KR_STRIDE = 2080;

constexpr size_t WS_XNF = 0;
constexpr size_t WS_XNT = 2u << 20;
constexpr size_t WS_KR  = 4u << 20;
constexpr size_t WS_KSC = 8u << 20;
constexpr size_t WS_W1  = 9u << 20;
constexpr size_t WS_W2  = 13u << 20;
constexpr size_t WS_HF  = 16u << 20;
constexpr size_t WS_YT  = 24u << 20;

__device__ __forceinline__ float bf2f(u16 v) { return __uint_as_float(((unsigned)v) << 16); }
__device__ __forceinline__ float bflo(unsigned v) { return __uint_as_float(v << 16); }
__device__ __forceinline__ float bfhi(unsigned v) { return __uint_as_float(v & 0xffff0000u); }
__device__ __forceinline__ u16 f2bf(float f) { __bf16 b = (__bf16)f; return __builtin_bit_cast(u16, b); }
__device__ __forceinline__ float rbf(float f) { return bf2f(f2bf(f)); }
__device__ __forceinline__ unsigned pack_bf(float lo, float hi) {
    return (unsigned)f2bf(lo) | ((unsigned)f2bf(hi) << 16);
}
__device__ __forceinline__ u16 f2h(float f) { _Float16 h = (_Float16)f; return __builtin_bit_cast(u16, h); }
__device__ __forceinline__ unsigned pack_h(float lo, float hi) {
    return (unsigned)f2h(lo) | ((unsigned)f2h(hi) << 16);
}
__device__ __forceinline__ float wave_sum(float v) {
#pragma unroll
    for (int o = 32; o > 0; o >>= 1) v += __shfl_xor(v, o, 64);
    return v;
}

constexpr int K1_W1_BLOCKS = 256, K1_W2_BLOCKS = 112, K1_LN_BLOCKS = 64, K1_KG_BLOCKS = 512;
constexpr int K1_GRID = K1_W1_BLOCKS + K1_W2_BLOCKS + K1_LN_BLOCKS + K1_KG_BLOCKS;
constexpr int K1_SMEM = 46 * 1024;

__device__ __forceinline__ void k1_w1(int b, const u16* __restrict__ wg, const u16* __restrict__ wu,
                                      u16* __restrict__ W1, char* smem) {
    const int nb = b >> 4, ks = b & 15, tid = threadIdx.x;
    u16* tile = (u16*)smem;
    for (int c = tid; c < 896; c += 256) {
        int gu = c / 448, rem = c - gu * 448, r = rem / 14, ch = rem - r * 14;
        const u16* src = (gu ? wu : wg) + (size_t)(32 * ks + r) * F_ + 112 * nb + ch * 8;
        u32x4 v = *(const u32x4*)src;
        *(u32x4*)(tile + (gu * 32 + r) * 120 + ch * 8) = v;
    }
    __syncthreads();
    u16* dst = W1 + (size_t)(nb * 16 + ks) * 14 * 512;
    for (int o = tid; o < 896; o += 256) {
        int t = o >> 6, lane = o & 63, fr = lane & 15, q = lane >> 4, tl = t >> 1, gu = t & 1;
        const u16* s = tile + (gu * 32 + 8 * q) * 120 + 16 * tl + fr;
        u32x4 v;
#pragma unroll
        for (int j = 0; j < 4; ++j) v[j] = (unsigned)s[(2 * j) * 120] | ((unsigned)s[(2 * j + 1) * 120] << 16);
        *(u32x4*)(dst + o * 8) = v;
    }
}

__device__ __forceinline__ void k1_w2(int b, const u16* __restrict__ wd, u16* __restrict__ W2, char* smem) {
    const int ks = b >> 1, dh = b & 1, tid = threadIdx.x;
    u16* tile = (u16*)smem;
    for (int c = tid; c < 1024; c += 256) {
        int r = c >> 5, ch = c & 31;
        u32x4 v = *(const u32x4*)(wd + (size_t)(32 * ks + r) * D_ + 256 * dh + ch * 8);
        *(u32x4*)(tile + r * 264 + ch * 8) = v;
    }
    __syncthreads();
    for (int o = tid; o < 1024; o += 256) {
        int dbl = o >> 8, t = (o >> 6) & 3, lane = o & 63, r = lane & 15, q = lane >> 4;
        int dl = 64 * dbl + 16 * (r >> 2) + 4 * t + (r & 3);
        const u16* s = tile + (4 * q) * 264 + dl;
        u32x4 v;
        v[0] = (unsigned)s[0 * 264] | ((unsigned)s[1 * 264] << 16);
        v[1] = (unsigned)s[2 * 264] | ((unsigned)s[3 * 264] << 16);
        v[2] = (unsigned)s[16 * 264] | ((unsigned)s[17 * 264] << 16);
        v[3] = (unsigned)s[18 * 264] | ((unsigned)s[19 * 264] << 16);
        int db = 4 * dh + dbl;
        *(u32x4*)(W2 + ((size_t)(db * 56 + ks) * 4 + t) * 512 + lane * 8) = v;
    }
}

__device__ __forceinline__ void k1_ln(int b, const u16* __restrict__ x, u16* __restrict__ xnF,
                                      u16* __restrict__ xnT, char* smem) {
    const int tid = threadIdx.x, w = tid >> 6, lane = tid & 63;
    constexpr int TS = 516;
    u16* tile = (u16*)smem;
    const int l0 = b * 32;
    u32x4 raw[8];
#pragma unroll
    for (int i = 0; i < 8; ++i) raw[i] = *(const u32x4*)(x + (size_t)(l0 + 8 * w + i) * D_ + lane * 8);
#pragma unroll
    for (int i = 0; i < 8; ++i) {
        float v[8];
#pragma unroll
        for (int j = 0; j < 4; ++j) { v[2 * j] = bflo(raw[i][j]); v[2 * j + 1] = bfhi(raw[i][j]); }
        float s = 0.f;
#pragma unroll
        for (int j = 0; j < 8; ++j) s += v[j];
        s = wave_sum(s);
        const float mean = s * (1.0f / D_);
        float q2 = 0.f;
#pragma unroll
        for (int j = 0; j < 8; ++j) { float dlt = v[j] - mean; q2 += dlt * dlt; }
        q2 = wave_sum(q2);
        const float mb = rbf(mean);
        const float vb = rbf(q2 * (1.0f / D_));
        const float sd = rbf(__builtin_sqrtf(rbf(vb + 1e-5f)));
        float o[8];
#pragma unroll
        for (int j = 0; j < 8; ++j) o[j] = rbf(rbf(v[j] - mb) / sd);
        const int row = l0 + 8 * w + i;
        u32x4 pb, ph;
#pragma unroll
        for (int j = 0; j < 4; ++j) { pb[j] = pack_bf(o[2 * j], o[2 * j + 1]); ph[j] = pack_h(o[2 * j], o[2 * j + 1]); }
        *(u32x4*)(xnF + ((size_t)((row >> 4) * 16 + (lane >> 2)) * 64 + (lane & 3) * 16 + (row & 15)) * 8) = pb;
        u16* tp = tile + (8 * w + i) * TS + lane * 8;
        *(u32x2*)(tp) = u32x2{ph[0], ph[1]};
        *(u32x2*)(tp + 4) = u32x2{ph[2], ph[3]};
    }
    __syncthreads();
#pragma unroll
    for (int it = 0; it < 8; ++it) {
        int d = it * 64 + w * 16 + (lane >> 2), part = lane & 3;
        const u16* s = tile + (8 * part) * TS + d;
        u32x4 v;
#pragma unroll
        for (int j = 0; j < 4; ++j) v[j] = (unsigned)s[(2 * j) * TS] | ((unsigned)s[(2 * j + 1) * TS] << 16);
        *(u32x4*)(xnT + (size_t)d * L_ + l0 + 8 * part) = v;
    }
}

__device__ __forceinline__ int bset_val(int i) {
    if (i < 16) return i;
    int s = (i - 8) >> 3;
    if (s > 3) s = 3;
    return (8 << s) + ((i - 8 - 8 * s) << s);
}
__device__ __forceinline__ int bset_idx(int e) {
    if (e < 16) return e;
    int s = (31 - __builtin_clz(e)) - 3;
    return 8 + 8 * s + ((e - (8 << s)) >> s);
}

__device__ __forceinline__ void k1_kgen(int d, const u16* __restrict__ lar, const u16* __restrict__ aim,
                                        const u16* __restrict__ cre, const u16* __restrict__ cim,
                                        const u16* __restrict__ ldt, u16* __restrict__ Kr,
                                        float* __restrict__ ksc, char* smem) {
    const int tid = threadIdx.x, w = tid >> 6, lane = tid & 63;
    constexpr int RS = 528;
    char* Ut = smem;
    char* Vt = smem + 41 * RS;
    u16* Kd = (u16*)(smem + 81 * RS + 16);
    const float dt = rbf(__expf(bf2f(ldt[d])));
    float sabs;
    {
        float a0 = fabsf(bf2f(cre[d * NS_ + lane])) + fabsf(bf2f(cim[d * NS_ + lane]));
        float a1 = fabsf(bf2f(cre[d * NS_ + 64 + lane])) + fabsf(bf2f(cim[d * NS_ + 64 + lane]));
        sabs = wave_sum(a0 + a1) * dt;
    }
    int ex = (int)((__float_as_uint(sabs) >> 23) & 0xff);
    int se = 127 + 14 - (ex - 126);
    se = se < 1 ? 1 : (se > 254 ? 254 : se);
    const float sc = __uint_as_float((unsigned)se << 23);
    const int n = (w & 1) * 64 + lane;
    const float ar = -rbf(__expf(bf2f(lar[d * NS_ + n])));
    const float zr = ar * dt, zi = bf2f(aim[d * NS_ + n]) * dt;
    const float zr2 = zr * 1.4426950408889634f;
    const float zi2 = zi * 0.15915494309189535f;
    if (w < 2) {
#pragma unroll 4
        for (int i = 0; i < 40; ++i) {
            float p = (float)bset_val(i);
            float mag = __builtin_amdgcn_exp2f(zr2 * p);
            float ph = __builtin_amdgcn_fractf(zi2 * p);
            float c = __builtin_amdgcn_cosf(ph), s = __builtin_amdgcn_sinf(ph);
            *(unsigned*)(Vt + i * RS + n * 4) = pack_h(mag * c, mag * s);
        }
    } else {
        const float cr = bf2f(cre[d * NS_ + n]) * dt * sc, ci = bf2f(cim[d * NS_ + n]) * dt * sc;
#pragma unroll 4
        for (int i = 0; i < 41; ++i) {
            float p = (float)(16 * bset_val(i));
            float mag = __builtin_amdgcn_exp2f(zr2 * p);
            float ph = __builtin_amdgcn_fractf(zi2 * p);
            float c = __builtin_amdgcn_cosf(ph) * mag, s = __builtin_amdgcn_sinf(ph) * mag;
            float ur = cr * c - ci * s, ui = cr * s + ci * c;
            *(unsigned*)(Ut + i * RS + n * 4) = pack_h(ur, -ui);
        }
    }
    __syncthreads();
    {
        const int r = lane & 15, q = lane >> 4;
        const int urow = (w == 0) ? r : (8 + 8 * w + r);
        const int vrow = bset_idx(r << w);
        const char* ua = Ut + urow * RS + q * 16;
        const char* vb = Vt + vrow * RS + q * 16;
        f32x4 acc = {0.f, 0.f, 0.f, 0.f};
#pragma unroll
        for (int ks = 0; ks < 8; ++ks) {
            f16x8 a = *(const f16x8*)(ua + ks * 64);
            f16x8 b = *(const f16x8*)(vb + ks * 64);
            acc = __builtin_amdgcn_mfma_f32_16x16x32_f16(a, b, acc, 0, 0, 0);
        }
#pragma unroll
        for (int g = 0; g < 4; ++g) Kd[w * 256 + (4 * q + g) * 16 + r] = f2h(acc[g]);
    }
    __syncthreads();
    for (int c = tid; c < KR_STRIDE / 8; c += 256) {
        u32x4 v;
#pragma unroll
        for (int j = 0; j < 8; ++j) {
            int t = 8 * c + j;
            int lag = 2047 - t;
            unsigned val = 0;
            if (lag >= 0) {
                int p = (int)rbf((float)lag);
                int idx;
                if (p < 256) idx = p;
                else {
                    int e = (31 - __builtin_clz(p)) - 7;
                    e = e > 3 ? 3 : e;
                    idx = e * 256 + ((p - (128 << e)) >> e);
                }
                val = Kd[idx];
            }
            if (j & 1) v[j >> 1] |= val << 16; else v[j >> 1] = val;
        }
        *(u32x4*)(Kr + (size_t)d * KR_STRIDE + 8 * c) = v;
    }
    if (tid == 0) ksc[d] = 2.0f / sc;
}

__global__ __launch_bounds__(256) void k1_prep(const u16* __restrict__ x, const u16* __restrict__ lar,
                                               const u16* __restrict__ aim, const u16* __restrict__ cre,
                                               const u16* __restrict__ cim, const u16* __restrict__ ldt,
                                               const u16* __restrict__ wg, const u16* __restrict__ wu,
                                               const u16* __restrict__ wd, char* __restrict__ ws) {
    __shared__ __attribute__((aligned(16))) char smem[K1_SMEM];
    int b = blockIdx.x;
    if (b < K1_W1_BLOCKS) { k1_w1(b, wg, wu, (u16*)(ws + WS_W1), smem); return; }
    b -= K1_W1_BLOCKS;
    if (b < K1_W2_BLOCKS) { k1_w2(b, wd, (u16*)(ws + WS_W2), smem); return; }
    b -= K1_W2_BLOCKS;
    if (b < K1_LN_BLOCKS) { k1_ln(b, x, (u16*)(ws + WS_XNF), (u16*)(ws + WS_XNT), smem); return; }
    b -= K1_LN_BLOCKS;
    k1_kgen(b, lar, aim, cre, cim, ldt, (u16*)(ws + WS_KR), (float*)(ws + WS_KSC), smem);
}

constexpr int G1_STAGE = 22 * 1024;
constexpr int CV_COPY = 4160;
constexpr int CV_KCH = 8 * CV_COPY;
constexpr int CV_XOFF = 2 * CV_KCH;
constexpr int CV_XCH = 4608;
constexpr int K2_SMEM = CV_XOFF + 2 * CV_XCH;

__device__ __forceinline__ float gelu_tanh(float x) {
    float u = 0.7978845608028654f * (x + 0.044715f * x * x * x);
    float e = __expf(2.0f * u);
    float t = 1.0f - 2.0f * __builtin_amdgcn_rcpf(e + 1.0f);
    return 0.5f * x * (1.0f + t);
}

__device__ __forceinline__ void k2_gemm1(int bid, const u16* __restrict__ xnF, const u16* __restrict__ W1,
                                         u16* __restrict__ hF, char* smem) {
    const int tid = threadIdx.x, lane = tid & 63;
    const int w = __builtin_amdgcn_readfirstlane(tid >> 6);
    const int xcd = bid & 7, i5 = bid >> 3;
    const int mb = (xcd & 1) * 8 + (i5 & 7), nb = (xcd >> 1) * 4 + (i5 >> 3);
    const u16* wsrc = W1 + (size_t)nb * 16 * 14 * 512 + lane * 8;
    const u16* xsrc = xnF + (size_t)mb * 8 * 16 * 512 + lane * 8;
    auto stage = [&](int ks, int buf) {
#pragma unroll
        for (int i = 0; i < 6; ++i) {
            int idx = w + 4 * i;
            if (idx >= 22) idx -= 4;
            const u16* src = idx < 14 ? wsrc + (size_t)(ks * 14 + idx) * 512
                                      : xsrc + (size_t)((idx - 14) * 16 + ks) * 512;
            __builtin_amdgcn_global_load_lds((const unsigned*)src,
                                             (unsigned*)(smem + buf * G1_STAGE + idx * 1024 + lane * 16), 16, 0, 0);
        }
    };
    f32x4 acc[7][2][2];
#pragma unroll
    for (int t = 0; t < 7; ++t)
#pragma unroll
        for (int g = 0; g < 2; ++g)
#pragma unroll
            for (int i = 0; i < 2; ++i) acc[t][g][i] = f32x4{0.f, 0.f, 0.f, 0.f};
    stage(0, 0);
    stage(1, 1);
#pragma unroll 1
    for (int ks = 0; ks < 16; ++ks) {
        if (ks < 15) asm volatile("s_waitcnt vmcnt(6)" ::: "memory");
        else asm volatile("s_waitcnt vmcnt(0)" ::: "memory");
        __builtin_amdgcn_s_barrier();
        int buf = ks % 3;
        if (ks + 2 < 16) stage(ks + 2, (ks + 2) % 3);
        const char* sb = smem + buf * G1_STAGE + lane * 16;
        bf16x8 xb[2];
#pragma unroll
        for (int i = 0; i < 2; ++i) xb[i] = *(const bf16x8*)(sb + (14 + 2 * w + i) * 1024);
#pragma unroll
        for (int t = 0; t < 14; ++t) {
            bf16x8 a = *(const bf16x8*)(sb + t * 1024);
#pragma unroll
            for (int i = 0; i < 2; ++i)
                acc[t >> 1][t & 1][i] = __builtin_amdgcn_mfma_f32_16x16x32_bf16(a, xb[i], acc[t >> 1][t & 1][i], 0, 0, 0);
        }
    }
#pragma unroll
    for (int t = 0; t < 7; ++t) {
        const int T = 7 * nb + t;
#pragma unroll
        for (int i = 0; i < 2; ++i) {
            float hv[4];
#pragma unroll
            for (int r = 0; r < 4; ++r) {
                float g = rbf(acc[t][0][i][r]), u = rbf(acc[t][1][i][r]);
                hv[r] = rbf(gelu_tanh(g)) * u;
            }
            u32x2 p = {pack_bf(hv[0], hv[1]), pack_bf(hv[2], hv[3])};
            const int lt = 8 * mb + 2 * w + i;
            *(u32x2*)((char*)hF + ((size_t)(lt * 56 + (T >> 1)) * 64 + lane) * 16 + (T & 1) * 8) = p;
        }
    }
}

__device__ __forceinline__ void k2_conv(int cb, const u16* __restrict__ xnT, const u16* __restrict__ Kr,
                                        const float* __restrict__ ksc, u16* __restrict__ yT, char* smem) {
    const int tid = threadIdx.x, lane = tid & 63;
    const int w = __builtin_amdgcn_readfirstlane(tid >> 6);
    const int d0 = 2 * cb;
#pragma unroll
    for (int ch = 0; ch < 2; ++ch) {
        const u16* kp = Kr + (size_t)(d0 + ch) * KR_STRIDE;
        for (int b = tid; b < 257; b += 256) {
            u32x4 lo = *(const u32x4*)(kp + 8 * b), hi = *(const u32x4*)(kp + 8 * b + 8);
            unsigned wd[8] = {lo[0], lo[1], lo[2], lo[3], hi[0], hi[1], hi[2], hi[3]};
            char* dst = smem + ch * CV_KCH + b * 16;
#pragma unroll
            for (int e = 0; e < 4; ++e) {
                u32x4 ev = {wd[e], wd[e + 1], wd[e + 2], wd[e + 3]};
                *(u32x4*)(dst + (2 * e) * CV_COPY) = ev;
                u32x4 od;
#pragma unroll
                for (int j = 0; j < 4; ++j) od[j] = __builtin_amdgcn_alignbit(wd[e + j + 1], wd[e + j], 16);
                *(u32x4*)(dst + (2 * e + 1) * CV_COPY) = od;
            }
        }
        char* xp = smem + CV_XOFF + ch * CV_XCH;
        if (tid < 32) *(u32x4*)(xp + tid * 16) = u32x4{0u, 0u, 0u, 0u};
        *(u32x4*)(xp + 512 + tid * 16) = *(const u32x4*)(xnT + (size_t)(d0 + ch) * L_ + tid * 8);
    }
    __syncthreads();
    const int ch = w >> 1, hw = w & 1;
    const int j = lane & 15, q = lane >> 4;
    const char* ka = smem + ch * CV_KCH + ((7 - j) & 7) * CV_COPY + (253 + q - (j >> 3)) * 16;
    const char* xb = smem + CV_XOFF + ch * CV_XCH + (256 + 16 * j - 16 + 8 * q) * 2;
    f32x4 acc[8];
#pragma unroll
    for (int i = 0; i < 8; ++i) acc[i] = f32x4{0.f, 0.f, 0.f, 0.f};
#pragma unroll 1
    for (int ri = 0; ri < 4; ++ri) {
        const int rho = hw * 4 + ri;
        f16x8 bfr[8];
#pragma unroll
        for (int c = 0; c < 8; ++c) bfr[c] = *(const f16x8*)(xb + c * 512 - rho * 64);
#pragma unroll
        for (int mm = 0; mm < 8; ++mm) {
            f16x8 a = *(const f16x8*)(ka - (8 * mm + rho) * 64);
#pragma unroll
            for (int ct = mm; ct < 8; ++ct)
                acc[ct] = __builtin_amdgcn_mfma_f32_16x16x32_f16(a, bfr[ct - mm], acc[ct], 0, 0, 0);
        }
    }
    __syncthreads();
    {
        f32x4* ex = (f32x4*)(smem + w * 4096) + lane;
        if (hw) {
#pragma unroll
            for (int i = 0; i < 4; ++i) { ex[i * 64] = acc[i]; acc[i] = acc[4 + i]; }
        } else {
#pragma unroll
            for (int i = 0; i < 4; ++i) ex[i * 64] = acc[4 + i];
        }
    }
    __syncthreads();
    {
        const f32x4* ex = (const f32x4*)(smem + (w ^ 1) * 4096) + lane;
        const float sc = ksc[d0 + ch];
        u16* yp = yT + (size_t)(d0 + ch) * L_ + 16 * j + 4 * q;
#pragma unroll
        for (int i = 0; i < 4; ++i) {
            f32x4 o = ex[i * 64];
            f32x4 m = acc[i];
            float v0 = (o[0] + m[0]) * sc, v1 = (o[1] + m[1]) * sc, v2 = (o[2] + m[2]) * sc, v3 = (o[3] + m[3]) * sc;
            u32x2 p = {pack_bf(v0, v1), pack_bf(v2, v3)};
            *(u32x2*)(yp + 256 * (4 * hw + i)) = p;
        }
    }
}

__global__ __launch_bounds__(256, 2) void k2_main(char* __restrict__ ws) {
    __shared__ __attribute__((aligned(16))) char smem[K2_SMEM];
    const int b = blockIdx.x;
    if (b < 256) k2_gemm1(b, (const u16*)(ws + WS_XNF), (const u16*)(ws + WS_W1), (u16*)(ws + WS_HF), smem);
    else k2_conv(b - 256, (const u16*)(ws + WS_XNT), (const u16*)(ws + WS_KR), (const float*)(ws + WS_KSC),
                 (u16*)(ws + WS_YT), smem);
}

constexpr int K3_RED = 4 * 3 * 4096 * 1;
constexpr int K3_YOFF = 64 * 1024;
constexpr int K3_YS = 72;
constexpr int K3_SMEM = K3_YOFF + 64 * K3_YS * 2;

__global__ __launch_bounds__(256, 1) void k3_main(const u16* __restrict__ x, const u16* __restrict__ dskip,
                                                  const char* __restrict__ ws, u16* __restrict__ out) {
    __shared__ __attribute__((aligned(16))) char smem[K3_SMEM];
    const int tid = threadIdx.x, lane = tid & 63;
    const int w = __builtin_amdgcn_readfirstlane(tid >> 6);
    const int bid = blockIdx.x, xcd = bid & 7, i5 = bid >> 3;
    const int lb = 4 * xcd + (i5 & 3), db = i5 >> 2;
    const u16* W2 = (const u16*)(ws + WS_W2);
    const u16* hF = (const u16*)(ws + WS_HF);
    const u16* yT = (const u16*)(ws + WS_YT);
    const u16* xnF = (const u16*)(ws + WS_XNF);
    const int r = lane & 15, q = lane >> 4;
    u32x4 yv[2];
#pragma unroll
    for (int i = 0; i < 2; ++i) {
        int c = tid + 256 * i;
        yv[i] = *(const u32x4*)(yT + (size_t)(64 * db + (c >> 3)) * L_ + 64 * lb + 8 * (c & 7));
    }
    const int lrow = 64 * lb + 16 * w + r;
    const int dcol = 64 * db + 16 * q;
    u32x4 xv[2], nv[2], dv[2];
#pragma unroll
    for (int i = 0; i < 2; ++i) {
        xv[i] = *(const u32x4*)(x + (size_t)lrow * D_ + dcol + 8 * i);
        int dd = dcol + 8 * i;
        nv[i] = *(const u32x4*)(xnF + ((size_t)((lrow >> 4) * 16 + (dd >> 5)) * 64 + ((dd >> 3) & 3) * 16 + (lrow & 15)) * 8);
        dv[i] = *(const u32x4*)(dskip + dd);
    }
    f32x4 acc[4][4];
#pragma unroll
    for (int t = 0; t < 4; ++t)
#pragma unroll
        for (int i = 0; i < 4; ++i) acc[t][i] = f32x4{0.f, 0.f, 0.f, 0.f};
    const u16* wp = W2 + ((size_t)(db * 56 + 14 * w) * 4) * 512 + lane * 8;
    const u16* hp = hF + ((size_t)(4 * lb) * 56 + 14 * w) * 512 + lane * 8;
    constexpr int PD = 4;
    bf16x8 wf[PD][4], hf[PD][4];
#pragma unroll
    for (int s = 0; s < PD; ++s) {
#pragma unroll
        for (int t = 0; t < 4; ++t) wf[s][t] = *(const bf16x8*)(wp + (s * 4 + t) * 512);
#pragma unroll
        for (int i = 0; i < 4; ++i) hf[s][i] = *(const bf16x8*)(hp + (i * 56 + s) * 512);
    }
#pragma unroll
    for (int k = 0; k < 14; ++k) {
        const int s = k % PD;
#pragma unroll
        for (int t = 0; t < 4; ++t)
#pragma unroll
            for (int i = 0; i < 4; ++i)
                acc[t][i] = __builtin_amdgcn_mfma_f32_16x16x32_bf16(wf[s][t], hf[s][i], acc[t][i], 0, 0, 0);
        if (k + PD < 14) {
#pragma unroll
            for (int t = 0; t < 4; ++t) wf[s][t] = *(const bf16x8*)(wp + ((k + PD) * 4 + t) * 512);
#pragma unroll
            for (int i = 0; i < 4; ++i) hf[s][i] = *(const bf16x8*)(hp + (i * 56 + k + PD) * 512);
        }
    }
    {
        u16* yt = (u16*)(smem + K3_YOFF);
#pragma unroll
        for (int i = 0; i < 2; ++i) {
            int c = tid + 256 * i, dl = c >> 3, lp = 8 * (c & 7);
#pragma unroll
            for (int j = 0; j < 4; ++j) {
                yt[(lp + 2 * j) * K3_YS + dl] = (u16)(yv[i][j] & 0xffff);
                yt[(lp + 2 * j + 1) * K3_YS + dl] = (u16)(yv[i][j] >> 16);
            }
        }
    }
    {
        f32x4* red = (f32x4*)smem;
#pragma unroll
        for (int i = 0; i < 4; ++i) {
            if (i != w) {
#pragma unroll
                for (int t = 0; t < 4; ++t) red[((i * 4 + w) * 4 + t) * 64 + lane] = acc[t][i];
            }
        }
    }
    __syncthreads();
    f32x4 f[4];
    {
        const f32x4* red = (const f32x4*)smem;
#pragma unroll
        for (int t = 0; t < 4; ++t) {
            f32x4 own = w == 0 ? acc[t][0] : (w == 1 ? acc[t][1] : (w == 2 ? acc[t][2] : acc[t][3]));
            f[t] = own;
        }
#pragma unroll
        for (int sw = 0; sw < 4; ++sw) {
            if (sw != w) {
#pragma unroll
                for (int t = 0; t < 4; ++t) f[t] += red[((w * 4 + sw) * 4 + t) * 64 + lane];
            }
        }
    }
    const u16* yt = (const u16*)(smem + K3_YOFF) + (16 * w + r) * K3_YS + 16 * q;
    u32x4 yy[2];
    yy[0] = *(const u32x4*)(yt);
    yy[1] = *(const u32x4*)(yt + 8);
#pragma unroll
    for (int i = 0; i < 2; ++i) {
        u32x4 ov;
#pragma unroll
        for (int jj = 0; jj < 4; ++jj) {
            float res[2];
#pragma unroll
            for (int hh = 0; hh < 2; ++hh) {
                const int e = 8 * i + 2 * jj + hh;
                float fv = rbf(f[e >> 2][e & 3]);
                float g = rbf(fv * __builtin_amdgcn_rcpf(1.0f + __expf(-fv)));
                float xn = hh ? bfhi(nv[i][jj]) : bflo(nv[i][jj]);
                float ds = hh ? bfhi(dv[i][jj]) : bflo(dv[i][jj]);
                float yv1 = hh ? bfhi(yy[i][jj]) : bflo(yy[i][jj]);
                float xx = hh ? bfhi(xv[i][jj]) : bflo(xv[i][jj]);
                float s = rbf(yv1 + rbf(xn * ds));
                res[hh] = xx + rbf(s * g);
            }
            ov[jj] = pack_bf(res[0], res[1]);
        }
        *(u32x4*)(out + (size_t)lrow * D_ + dcol + 8 * i) = ov;
    }
}

extern "C" void kernel_launch(void* const* d_in, const int* in_sizes, int n_in,
                              void* d_out, int out_size, void* d_ws, size_t ws_size,
                              hipStream_t stream) {
    const u16* x   = (const u16*)d_in[0];
    const u16* lar = (const u16*)d_in[1];
    const u16* aim = (const u16*)d_in[2];
    const u16* cre = (const u16*)d_in[3];
    const u16* cim = (const u16*)d_in[4];
    const u16* ldt = (const u16*)d_in[5];
    const u16* dsk = (const u16*)d_in[6];
    const u16* wg  = (const u16*)d_in[8];
    const u16* wu  = (const u16*)d_in[9];
    const u16* wd  = (const u16*)d_in[10];
    char* ws = (char*)d_ws;
    k1_prep<<<K1_GRID, 256, 0, stream>>>(x, lar, aim, cre, cim, ldt, wg, wu, wd, ws);
    k2_main<<<512, 256, 0, stream>>>(ws);
    k3_main<<<256, 256, 0, stream>>>(x, dsk, ws, (u16*)d_out);
}
```

```cpp
#include <hip/hip_runtime.h>
#include <hip/hip_bf16.h>
#include <stdint.h>

typedef __attribute__((ext_vector_type(8))) __bf16 bf16x8;
typedef __attribute__((ext_vector_type(8))) _Float16 f16x8;
typedef __attribute__((ext_vector_type(2))) _Float16 f16x2;
typedef __attribute__((ext_vector_type(4))) float f32x4;
typedef __attribute__((ext_vector_type(4))) unsigned int u32x4;
typedef __attribute__((ext_vector_type(2))) unsigned int u32x2;
typedef unsigned short u16;

constexpr int L_ = 2048, D_ = 512, NS_ = 128, F_ = 1792;
constexpr int KR_STRIDE = 2080;

constexpr size_t WS_XNF = 0;
constexpr size_t WS_XNT = 2u << 20;
constexpr size_t WS_KR  = 4u << 20;
constexpr size_t WS_KSC = 8u << 20;
constexpr size_t WS_W1  = 9u << 20;
constexpr size_t WS_W2  = 13u << 20;
constexpr size_t WS_HF  = 16u << 20;
constexpr size_t WS_YT  = 24u << 20;

#define STAMP(ws_, kid, slot) do {} while (0)
__device__ __forceinline__ float bf2f(u16 v) { return __uint_as_float(((unsigned)v) << 16); }
__device__ __forceinline__ float bflo(unsigned v) { return __uint_as_float(v << 16); }
__device__ __forceinline__ float bfhi(unsigned v) { return __uint_as_float(v & 0xffff0000u); }
__device__ __forceinline__ u16 f2bf(float f) { __bf16 b = (__bf16)f; return __builtin_bit_cast(u16, b); }
__device__ __forceinline__ float rbf(float f) { return bf2f(f2bf(f)); }
__device__ __forceinline__ unsigned pack_bf(float lo, float hi) {
    return (unsigned)f2bf(lo) | ((unsigned)f2bf(hi) << 16);
}
__device__ __forceinline__ u16 f2h(float f) { _Float16 h = (_Float16)f; return __builtin_bit_cast(u16, h); }
__device__ __forceinline__ unsigned pack_h(float lo, float hi) {
    return (unsigned)f2h(lo) | ((unsigned)f2h(hi) << 16);
}
__device__ __forceinline__ float wave_sum(float v) {
#pragma unroll
    for (int o = 32; o > 0; o >>= 1) v += __shfl_xor(v, o, 64);
    return v;
}

constexpr int K1_KG_BLOCKS = 512, K1_LN_BLOCKS = 64, K1_W1_BLOCKS = 128, K1_W2_BLOCKS = 56;
constexpr int K1_GRID = K1_W1_BLOCKS + K1_W2_BLOCKS + K1_LN_BLOCKS + K1_KG_BLOCKS;
constexpr int K1_SMEM = 46 * 1024;
#define SCHED_FENCE() __builtin_amdgcn_sched_barrier(0)

__device__ __forceinline__ void k1_w1(int b, const u16* __restrict__ wg, const u16* __restrict__ wu,
                                      u16* __restrict__ W1, char* smem) {
    const int nb = b >> 3, kp = b & 7, tid = threadIdx.x;
    u16* tile = (u16*)smem;
    u32x4 v[7];
#pragma unroll
    for (int i = 0; i < 7; ++i) {
        int c = tid + 256 * i;
        int gu = c / 896, rem = c - gu * 896, r = rem / 14, ch = rem - r * 14;
        v[i] = *(const u32x4*)((gu ? wu : wg) + (size_t)(64 * kp + r) * F_ + 112 * nb + ch * 8);
    }
    SCHED_FENCE();
#pragma unroll
    for (int i = 0; i < 7; ++i) {
        int c = tid + 256 * i;
        int gu = c / 896, rem = c - gu * 896, r = rem / 14, ch = rem - r * 14;
        *(u32x4*)(tile + (gu * 64 + r) * 120 + ch * 8) = v[i];
    }
    __syncthreads();
    u16* dst = W1 + (size_t)(nb * 16 + 2 * kp) * 14 * 512;
#pragma unroll
    for (int i = 0; i < 7; ++i) {
        int o = tid + 256 * i;
        int kk = o / 896, rem = o - kk * 896;
        int t = rem >> 6, lane = rem & 63, fr = lane & 15, q = lane >> 4, tl = t >> 1, gu = t & 1;
        const u16* sp = tile + (gu * 64 + 32 * kk + 8 * q) * 120 + 16 * tl + fr;
        u32x4 o4;
#pragma unroll
        for (int j = 0; j < 4; ++j) o4[j] = (unsigned)sp[(2 * j) * 120] | ((unsigned)sp[(2 * j + 1) * 120] << 16);
        *(u32x4*)(dst + o * 8) = o4;
    }
}

__device__ __forceinline__ void k1_w2(int ks, const u16* __restrict__ wd, u16* __restrict__ W2, char* smem) {
    const int tid = threadIdx.x;
    u16* tile = (u16*)smem;
    u32x4 v[8];
#pragma unroll
    for (int i = 0; i < 8; ++i) {
        int c = tid + 256 * i, r = c >> 6, ch = c & 63;
        v[i] = *(const u32x4*)(wd + (size_t)(32 * ks + r) * D_ + ch * 8);
    }
    SCHED_FENCE();
#pragma unroll
    for (int i = 0; i < 8; ++i) {
        int c = tid + 256 * i, r = c >> 6, ch = c & 63;
        *(u32x4*)(tile + r * 520 + ch * 8) = v[i];
    }
    __syncthreads();
#pragma unroll
    for (int db = 0; db < 8; ++db) {
        int t = tid >> 6, lane = tid & 63, r = lane & 15, q = lane >> 4;
        int dl = 64 * db + 16 * (r >> 2) + 4 * t + (r & 3);
        const u16* sp = tile + (4 * q) * 520 + dl;
        u32x4 o4;
        o4[0] = (unsigned)sp[0 * 520] | ((unsigned)sp[1 * 520] << 16);
        o4[1] = (unsigned)sp[2 * 520] | ((unsigned)sp[3 * 520] << 16);
        o4[2] = (unsigned)sp[16 * 520] | ((unsigned)sp[17 * 520] << 16);
        o4[3] = (unsigned)sp[18 * 520] | ((unsigned)sp[19 * 520] << 16);
        *(u32x4*)(W2 + ((size_t)(db * 56 + ks) * 4 + t) * 512 + lane * 8) = o4;
    }
}

__device__ __forceinline__ float row16_sum(float v) {
    v += __builtin_bit_cast(float, __builtin_amdgcn_update_dpp(0, __builtin_bit_cast(int, v), 0x128, 0xf, 0xf, false));
    v += __builtin_bit_cast(float, __builtin_amdgcn_update_dpp(0, __builtin_bit_cast(int, v), 0x124, 0xf, 0xf, false));
    v += __builtin_bit_cast(float, __builtin_amdgcn_update_dpp(0, __builtin_bit_cast(int, v), 0x122, 0xf, 0xf, false));
    v += __builtin_bit_cast(float, __builtin_amdgcn_update_dpp(0, __builtin_bit_cast(int, v), 0x121, 0xf, 0xf, false));
    return v;
}

__device__ __forceinline__ size_t xnf_off(int row, int chunk) {
    return ((size_t)(((row >> 5) * 16 + (chunk >> 2)) * 2 + ((row >> 4) & 1)) * 64 + (chunk & 3) * 16 + (row & 15)) * 8;
}

__device__ __forceinline__ void k1_ln(int b, const u16* __restrict__ x, u16* __restrict__ xnF,
                                      u16* __restrict__ xnT, char* smem) {
    const int tid = threadIdx.x, w = tid >> 6, lane = tid & 63, rr = lane >> 4, c = lane & 15;
    constexpr int TS = 516;
    u16* tile = (u16*)smem;
    const int l0 = b * 32;
    u32x4 raw[2][4];
#pragma unroll
    for (int bt = 0; bt < 2; ++bt)
#pragma unroll
        for (int i = 0; i < 4; ++i)
            raw[bt][i] = *(const u32x4*)(x + (size_t)(l0 + 8 * w + 4 * bt + rr) * D_ + (c + 16 * i) * 8);
    SCHED_FENCE();
#pragma unroll
    for (int bt = 0; bt < 2; ++bt) {
        float v[32];
#pragma unroll
        for (int i = 0; i < 4; ++i)
#pragma unroll
            for (int j = 0; j < 4; ++j) { v[8 * i + 2 * j] = bflo(raw[bt][i][j]); v[8 * i + 2 * j + 1] = bfhi(raw[bt][i][j]); }
        float s = 0.f;
#pragma unroll
        for (int j = 0; j < 32; ++j) s += v[j];
        s = row16_sum(s);
        const float mean = s * (1.0f / D_);
        float q2 = 0.f;
#pragma unroll
        for (int j = 0; j < 32; ++j) { float dlt = v[j] - mean; q2 += dlt * dlt; }
        q2 = row16_sum(q2);
        const float mb = rbf(mean);
        const float vb = rbf(q2 * (1.0f / D_));
        const float sd = rbf(__builtin_sqrtf(rbf(vb + 1e-5f)));
        const float rs = 1.0f / sd;
        const int rl = 8 * w + 4 * bt + rr, row = l0 + rl;
#pragma unroll
        for (int i = 0; i < 4; ++i) {
            u32x4 pb, ph;
#pragma unroll
            for (int j = 0; j < 4; ++j) {
                float o0 = rbf(rbf(v[8 * i + 2 * j] - mb) * rs), o1 = rbf(rbf(v[8 * i + 2 * j + 1] - mb) * rs);
                pb[j] = pack_bf(o0, o1); ph[j] = pack_h(o0, o1);
            }
            *(u32x4*)(xnF + xnf_off(row, c + 16 * i)) = pb;
            u16* tp = tile + rl * TS + (c + 16 * i) * 8;
            *(u32x2*)(tp) = u32x2{ph[0], ph[1]};
            *(u32x2*)(tp + 4) = u32x2{ph[2], ph[3]};
        }
    }
    __syncthreads();
#pragma unroll
    for (int it = 0; it < 8; ++it) {
        int d = it * 64 + w * 16 + (lane >> 2), part = lane & 3;
        const u16* sp = tile + (8 * part) * TS + d;
        u32x4 v;
#pragma unroll
        for (int j = 0; j < 4; ++j) v[j] = (unsigned)sp[(2 * j) * TS] | ((unsigned)sp[(2 * j + 1) * TS] << 16);
        *(u32x4*)(xnT + (size_t)d * L_ + l0 + 8 * part) = v;
    }
}

struct IdxTab { u16 v[KR_STRIDE]; };
constexpr IdxTab make_idx_tab() {
    IdxTab t{};
    for (int i = 0; i < KR_STRIDE; ++i) {
        int lag = 2047 - i;
        if (lag < 0) { t.v[i] = 1024; continue; }
        int p = lag;
        if (lag >= 256) {
            int s = 0;
            while ((lag >> s) >= 256) ++s;
            int step = 1 << s, rem = lag & (step - 1), base = lag - rem, half = step >> 1;
            if (rem > half || (rem == half && ((base >> s) & 1))) base += step;
            p = base;
        }
        int idx = p;
        if (p >= 256) {
            int e = 0;
            while ((p >> e) >= 256) ++e;
            if (e > 3) e = 3;
            idx = e * 256 + ((p - (128 << e)) >> e);
        }
        t.v[i] = (u16)idx;
    }
    return t;
}
__device__ const IdxTab kIdxTab = make_idx_tab();

__device__ __forceinline__ int bset_idx(int e) {
    if (e < 16) return e;
    int s = (31 - __builtin_clz(e)) - 3;
    return 8 + 8 * s + ((e - (8 << s)) >> s);
}
struct cplx { float r, i; };
__device__ __forceinline__ cplx cmul(cplx a, cplx b) { return cplx{a.r * b.r - a.i * b.i, a.r * b.i + a.i * b.r}; }
__device__ __forceinline__ cplx cexp_p(float zr2, float zi2, float p) {
    float mag = __builtin_amdgcn_exp2f(zr2 * p);
    float ph = __builtin_amdgcn_fractf(zi2 * p);
    return cplx{mag * __builtin_amdgcn_cosf(ph), mag * __builtin_amdgcn_sinf(ph)};
}

__device__ __forceinline__ void k1_kgen(int d, const u16* __restrict__ lar, const u16* __restrict__ aim,
                                        const u16* __restrict__ cre, const u16* __restrict__ cim,
                                        const u16* __restrict__ ldt, u16* __restrict__ Kr,
                                        float* __restrict__ ksc, char* smem) {
    const int tid = threadIdx.x, lane = tid & 63;
    const int w = __builtin_amdgcn_readfirstlane(tid >> 6);
    constexpr int RS = 528;
    char* Ut = smem;
    char* Vt = smem + 41 * RS;
    u16* Kd = (u16*)(smem + 81 * RS + 16);
    const int n = (w & 1) * 64 + lane;
    const u16 r_ldt = ldt[d];
    const u16 r_c0 = cre[d * NS_ + lane], r_c1 = cim[d * NS_ + lane], r_c2 = cre[d * NS_ + 64 + lane], r_c3 = cim[d * NS_ + 64 + lane];
    const u16 r_lar = lar[d * NS_ + n], r_aim = aim[d * NS_ + n];
    const u32x4 itab = *(const u32x4*)(kIdxTab.v + 8 * tid);
    u32x4 itab2 = u32x4{0u, 0u, 0u, 0u};
    if (tid < KR_STRIDE / 8 - 256) itab2 = *(const u32x4*)(kIdxTab.v + 8 * (256 + tid));
    SCHED_FENCE();
    const float dt = rbf(__expf(bf2f(r_ldt)));
    const float sabs = wave_sum(fabsf(bf2f(r_c0)) + fabsf(bf2f(r_c1)) + fabsf(bf2f(r_c2)) + fabsf(bf2f(r_c3))) * dt;
    int ex = (int)((__float_as_uint(sabs) >> 23) & 0xff);
    int se = 127 + 14 - (ex - 126);
    se = se < 1 ? 1 : (se > 254 ? 254 : se);
    const float sc = __uint_as_float((unsigned)se << 23);
    const float ar = -rbf(__expf(bf2f(r_lar)));
    const float zr = ar * dt, zi = bf2f(r_aim) * dt;
    const bool isU = w >= 2;
    const float pb = isU ? 16.0f : 1.0f;
    const float zr2 = zr * 1.4426950408889634f;
    const float zi2 = zi * 0.15915494309189535f;
    cplx e[8];
#pragma unroll
    for (int k = 0; k < 8; ++k) e[k] = cexp_p(zr2, zi2, pb * (float)(1 << k));
    cplx v[41];
    {
        const float crv = bf2f((w & 1) ? r_c2 : r_c0), civ = bf2f((w & 1) ? r_c3 : r_c1);
        v[0] = isU ? cplx{crv * dt * sc, civ * dt * sc} : cplx{1.0f, 0.0f};
    }
    v[1] = cmul(v[0], e[0]);
#pragma unroll
    for (int k = 0; k < 2; ++k) v[2 + k] = cmul(v[k], e[1]);
#pragma unroll
    for (int k = 0; k < 4; ++k) v[4 + k] = cmul(v[k], e[2]);
#pragma unroll
    for (int k = 0; k < 8; ++k) v[8 + k] = cmul(v[k], e[3]);
#pragma unroll
    for (int k = 0; k < 8; ++k) v[16 + k] = cmul(v[2 * k], e[4]);
#pragma unroll
    for (int k = 0; k < 8; ++k) v[24 + k] = cmul(v[k < 4 ? 4 * k : 16 + (4 * k - 16) / 2], e[5]);
#pragma unroll
    for (int k = 0; k < 8; ++k) {
        const int ee = 8 * k;
        const int src = ee < 16 ? ee : (ee < 32 ? 16 + (ee - 16) / 2 : 24 + (ee - 32) / 4);
        v[32 + k] = cmul(v[src], e[6]);
    }
    v[40] = cmul(v[0], e[7]);
    {
        char* tb = (isU ? Ut : Vt) + n * 4;
        const float sg = isU ? -1.0f : 1.0f;
#pragma unroll
        for (int i = 0; i < 40; ++i) *(unsigned*)(tb + i * RS) = pack_h(v[i].r, sg * v[i].i);
        if (isU) *(unsigned*)(tb + 40 * RS) = pack_h(v[40].r, sg * v[40].i);
    }
    if (tid == 0) Kd[1024] = 0;
    __syncthreads();
    {
        const int r = lane & 15, q = lane >> 4;
        const int urow = (w == 0) ? r : (8 + 8 * w + r);
        const int vrow = bset_idx(r << w);
        const char* ua = Ut + urow * RS + q * 16;
        const char* vb = Vt + vrow * RS + q * 16;
        f16x8 a[8], bb[8];
#pragma unroll
        for (int ks = 0; ks < 8; ++ks) { a[ks] = *(const f16x8*)(ua + ks * 64); bb[ks] = *(const f16x8*)(vb + ks * 64); }
        SCHED_FENCE();
        f32x4 acc0 = {0.f, 0.f, 0.f, 0.f}, acc1 = {0.f, 0.f, 0.f, 0.f};
#pragma unroll
        for (int ks = 0; ks < 8; ks += 2) {
            acc0 = __builtin_amdgcn_mfma_f32_16x16x32_f16(a[ks], bb[ks], acc0, 0, 0, 0);
            acc1 = __builtin_amdgcn_mfma_f32_16x16x32_f16(a[ks + 1], bb[ks + 1], acc1, 0, 0, 0);
        }
#pragma unroll
        for (int g = 0; g < 4; ++g) Kd[w * 256 + (4 * q + g) * 16 + r] = f2h(acc0[g] + acc1[g]);
    }
    __syncthreads();
    {
        u32x4 o;
#pragma unroll
        for (int j = 0; j < 4; ++j) o[j] = (unsigned)Kd[itab[j] & 0xffff] | ((unsigned)Kd[itab[j] >> 16] << 16);
        *(u32x4*)(Kr + (size_t)d * KR_STRIDE + 8 * tid) = o;
        if (tid < KR_STRIDE / 8 - 256) {
#pragma unroll
            for (int j = 0; j < 4; ++j) o[j] = (unsigned)Kd[itab2[j] & 0xffff] | ((unsigned)Kd[itab2[j] >> 16] << 16);
            *(u32x4*)(Kr + (size_t)d * KR_STRIDE + 8 * (256 + tid)) = o;
        }
    }
    if (tid == 0) ksc[d] = 2.0f / sc;
}

__global__ __launch_bounds__(256) void k1_prep(const u16* __restrict__ x, const u16* __restrict__ lar,
                                               const u16* __restrict__ aim, const u16* __restrict__ cre,
                                               const u16* __restrict__ cim, const u16* __restrict__ ldt,
                                               const u16* __restrict__ wg, const u16* __restrict__ wu,
                                               const u16* __restrict__ wd, char* __restrict__ ws) {
    __shared__ __attribute__((aligned(16))) char smem[K1_SMEM];
    int b = blockIdx.x;
    STAMP(ws, 0, 0);
#define K1_MASK 15
    if (b < K1_W1_BLOCKS) { if (K1_MASK & 1) k1_w1(b, wg, wu, (u16*)(ws + WS_W1), smem); STAMP(ws, 0, 3); return; }
    b -= K1_W1_BLOCKS;
    if (b < K1_W2_BLOCKS) { if (K1_MASK & 2) k1_w2(b, wd, (u16*)(ws + WS_W2), smem); STAMP(ws, 0, 3); return; }
    b -= K1_W2_BLOCKS;
    if (b < K1_LN_BLOCKS) { if (K1_MASK & 4) k1_ln(b, x, (u16*)(ws + WS_XNF), (u16*)(ws + WS_XNT), smem); STAMP(ws, 0, 3); return; }
    b -= K1_LN_BLOCKS;
    if (K1_MASK & 8) k1_kgen(b, lar, aim, cre, cim, ldt, (u16*)(ws + WS_KR), (float*)(ws + WS_KSC), smem);
    STAMP(ws, 0, 3);
}

constexpr int G1_STAGE = 22 * 1024;
constexpr int CV_COPY = 4160;
constexpr int CV_KCH = 8 * CV_COPY;
constexpr int CV_XOFF = 2 * CV_KCH;
constexpr int CV_XCH = 4608;
constexpr int K2_SMEM = CV_XOFF + 2 * CV_XCH;

__device__ __forceinline__ float gelu_tanh(float x) {
    float u = 0.7978845608028654f * (x + 0.044715f * x * x * x);
    float e = __expf(2.0f * u);
    float t = 1.0f - 2.0f * __builtin_amdgcn_rcpf(e + 1.0f);
    return 0.5f * x * (1.0f + t);
}

__device__ __forceinline__ void k2_gemm1(int bid, const u16* __restrict__ xnF, const u16* __restrict__ W1,
                                         u16* __restrict__ hF, char* smem) {
    const int tid = threadIdx.x, lane = tid & 63;
    const int w = __builtin_amdgcn_readfirstlane(tid >> 6);
    const int xcd = bid & 7, i5 = bid >> 3;
    const int mb = (xcd & 1) * 8 + (i5 & 7), nb = (xcd >> 1) * 4 + (i5 >> 3);
    STAMP(xnF, 1, 0);
    const int w0 = w < 2 ? 4 * w : (w == 2 ? 8 : 11);
    const int wl3 = w < 2 ? 3 : 2;
    const char* wsrc = (const char*)W1 + ((size_t)nb * 16 * 14 + w0) * 1024 + lane * 16;
    const char* xsrc = (const char*)xnF + ((size_t)(mb * 4 + w) * 16 * 2) * 1024 + lane * 16;
    char* wdst = smem + w0 * 1024 + lane * 16;
    char* xdst = smem + (14 + 2 * w) * 1024 + lane * 16;
    auto stage1 = [&](int ks, int i) {
        const int buf = ks % 3;
        if (i < 3) {
            __builtin_amdgcn_global_load_lds((const unsigned*)(wsrc + ks * 14 * 1024 + i * 1024),
                                             (unsigned*)(wdst + buf * G1_STAGE + i * 1024), 16, 0, 0);
        } else if (i == 3) {
            __builtin_amdgcn_global_load_lds((const unsigned*)(wsrc + ks * 14 * 1024 + wl3 * 1024),
                                             (unsigned*)(wdst + buf * G1_STAGE + wl3 * 1024), 16, 0, 0);
        } else {
            __builtin_amdgcn_global_load_lds((const unsigned*)(xsrc + ks * 2048 + (i - 4) * 1024),
                                             (unsigned*)(xdst + buf * G1_STAGE + (i - 4) * 1024), 16, 0, 0);
        }
    };
    f32x4 acc[7][2][2];
#pragma unroll
    for (int t = 0; t < 7; ++t)
#pragma unroll
        for (int g = 0; g < 2; ++g)
#pragma unroll
            for (int i = 0; i < 2; ++i) acc[t][g][i] = f32x4{0.f, 0.f, 0.f, 0.f};
    constexpr int LAG = 4, NSL = 14 + LAG;
    bf16x8 wf[NSL], xf[2][2];
    const char* rw = smem + lane * 16;
    const char* rx = smem + (14 + 2 * w) * 1024 + lane * 16;
#pragma unroll
    for (int s3 = 0; s3 < 3; ++s3)
#pragma unroll
        for (int i = 0; i < 6; ++i) stage1(s3, i);
    asm volatile("s_waitcnt vmcnt(12)" ::: "memory");
    __builtin_amdgcn_s_barrier();
    SCHED_FENCE();
    STAMP(xnF, 1, 1);
#pragma unroll
    for (int i = 0; i < 2; ++i) xf[0][i] = *(const bf16x8*)(rx + i * 1024);
#pragma unroll
    for (int j = 0; j < 14; ++j) wf[j] = *(const bf16x8*)(rw + j * 1024);
    SCHED_FENCE();
#pragma unroll
    for (int ks = 0; ks < 16; ++ks) {
        const int nbuf = ((ks + 1) % 3) * G1_STAGE;
        if (ks < 15) {
            if (ks < 14) asm volatile("s_waitcnt vmcnt(6) lgkmcnt(0)" ::: "memory");
            else asm volatile("s_waitcnt vmcnt(0) lgkmcnt(0)" ::: "memory");
            __builtin_amdgcn_s_barrier();
            SCHED_FENCE();
#pragma unroll
            for (int i = 0; i < 2; ++i) xf[(ks + 1) & 1][i] = *(const bf16x8*)(rx + nbuf + i * 1024);
#pragma unroll
            for (int j = 0; j < LAG; ++j) wf[(14 * (ks + 1) + j) % NSL] = *(const bf16x8*)(rw + nbuf + j * 1024);
            SCHED_FENCE();
        }
#pragma unroll
        for (int j = 0; j < 14; ++j) {
            const int sl = (14 * ks + j) % NSL;
#pragma unroll
            for (int i = 0; i < 2; ++i)
                acc[j >> 1][j & 1][i] = __builtin_amdgcn_mfma_f32_16x16x32_bf16(wf[sl], xf[ks & 1][i], acc[j >> 1][j & 1][i], 0, 0, 0);
            if (ks < 15 && j + LAG <= 13) wf[sl] = *(const bf16x8*)(rw + nbuf + (j + LAG) * 1024);
            if (ks + 3 < 16 && (j & 1) == 1 && (j >> 1) < 6) stage1(ks + 3, j >> 1);
            SCHED_FENCE();
        }
    }
    STAMP(xnF, 1, 2);
#pragma unroll
    for (int t = 0; t < 7; ++t) {
        const int T = 7 * nb + t;
#pragma unroll
        for (int i = 0; i < 2; ++i) {
            float hv[4];
#pragma unroll
            for (int r = 0; r < 4; ++r) {
                float g = rbf(acc[t][0][i][r]), u = rbf(acc[t][1][i][r]);
                hv[r] = rbf(gelu_tanh(g)) * u;
            }
            u32x2 p = {pack_bf(hv[0], hv[1]), pack_bf(hv[2], hv[3])};
            const int lt = 8 * mb + 2 * w + i;
            *(u32x2*)((char*)hF + ((size_t)(lt * 56 + (T >> 1)) * 64 + lane) * 16 + (T & 1) * 8) = p;
        }
    }
    STAMP(xnF, 1, 3);
}

__device__ __forceinline__ void k2_conv(int cb, const u16* __restrict__ xnT, const u16* __restrict__ Kr,
                                        const float* __restrict__ ksc, u16* __restrict__ yT, char* smem) {
    const int tid = threadIdx.x, lane = tid & 63;
    const int w = __builtin_amdgcn_readfirstlane(tid >> 6);
    const int d0 = 2 * cb;
    STAMP((const char*)xnT - WS_XNT, 1, 0);
#pragma unroll
    for (int ch = 0; ch < 2; ++ch) {
        const u16* kp = Kr + (size_t)(d0 + ch) * KR_STRIDE;
        for (int b = tid; b < 257; b += 256) {
            u32x4 lo = *(const u32x4*)(kp + 8 * b), hi = *(const u32x4*)(kp + 8 * b + 8);
            unsigned wd[8] = {lo[0], lo[1], lo[2], lo[3], hi[0], hi[1], hi[2], hi[3]};
            char* dst = smem + ch * CV_KCH + b * 16;
#pragma unroll
            for (int e = 0; e < 4; ++e) {
                u32x4 ev = {wd[e], wd[e + 1], wd[e + 2], wd[e + 3]};
                *(u32x4*)(dst + (2 * e) * CV_COPY) = ev;
                u32x4 od;
#pragma unroll
                for (int j = 0; j < 4; ++j) od[j] = __builtin_amdgcn_alignbit(wd[e + j + 1], wd[e + j], 16);
                *(u32x4*)(dst + (2 * e + 1) * CV_COPY) = od;
            }
        }
        char* xp = smem + CV_XOFF + ch * CV_XCH;
        if (tid < 32) *(u32x4*)(xp + tid * 16) = u32x4{0u, 0u, 0u, 0u};
        *(u32x4*)(xp + 512 + tid * 16) = *(const u32x4*)(xnT + (size_t)(d0 + ch) * L_ + tid * 8);
    }
    __syncthreads();
    STAMP((const char*)xnT - WS_XNT, 1, 1);
    const int ch = w >> 1, hw = w & 1;
    const int j = lane & 15, q = lane >> 4;
    const char* ka = smem + ch * CV_KCH + ((7 - j) & 7) * CV_COPY + (253 + q - (j >> 3)) * 16;
    const char* xb = smem + CV_XOFF + ch * CV_XCH + (256 + 16 * j - 16 + 8 * q) * 2;
    f32x4 acc[8];
#pragma unroll
    for (int i = 0; i < 8; ++i) acc[i] = f32x4{0.f, 0.f, 0.f, 0.f};
    {
        const char* kb = ka - hw * 4 * 64;
        const char* xq = xb - hw * 4 * 64;
        f16x8 bfr[2][8], af[3];
#pragma unroll
        for (int c = 0; c < 8; ++c) bfr[0][c] = *(const f16x8*)(xq + c * 512);
        af[0] = *(const f16x8*)(kb);
        af[1] = *(const f16x8*)(kb - 8 * 64);
        SCHED_FENCE();
#pragma unroll
        for (int ri = 0; ri < 4; ++ri) {
#pragma unroll
            for (int mm = 0; mm < 8; ++mm) {
                const int g = ri * 8 + mm;
                if (g + 2 < 32) {
                    const int g2 = g + 2;
                    af[g2 % 3] = *(const f16x8*)(kb - (8 * (g2 & 7) + (g2 >> 3)) * 64);
                }
                if (ri < 3) bfr[(ri + 1) & 1][mm] = *(const f16x8*)(xq + mm * 512 - (ri + 1) * 64);
#pragma unroll
                for (int ct = mm; ct < 8; ++ct)
                    acc[ct] = __builtin_amdgcn_mfma_f32_16x16x32_f16(af[g % 3], bfr[ri & 1][ct - mm], acc[ct], 0, 0, 0);
                SCHED_FENCE();
            }
        }
    }
    STAMP((const char*)xnT - WS_XNT, 1, 2);
    __syncthreads();
    {
        f32x4* ex = (f32x4*)(smem + w * 4096) + lane;
        if (hw) {
#pragma unroll
            for (int i = 0; i < 4; ++i) { ex[i * 64] = acc[i]; acc[i] = acc[4 + i]; }
        } else {
#pragma unroll
            for (int i = 0; i < 4; ++i) ex[i * 64] = acc[4 + i];
        }
    }
    __syncthreads();
    {
        const f32x4* ex = (const f32x4*)(smem + (w ^ 1) * 4096) + lane;
        const float sc = ksc[d0 + ch];
        u16* yp = yT + (size_t)(d0 + ch) * L_ + 16 * j + 4 * q;
#pragma unroll
        for (int i = 0; i < 4; ++i) {
            f32x4 o = ex[i * 64];
            f32x4 m = acc[i];
            float v0 = (o[0] + m[0]) * sc, v1 = (o[1] + m[1]) * sc, v2 = (o[2] + m[2]) * sc, v3 = (o[3] + m[3]) * sc;
            u32x2 p = {pack_bf(v0, v1), pack_bf(v2, v3)};
            *(u32x2*)(yp + 256 * (4 * hw + i)) = p;
        }
    }
    STAMP((const char*)xnT - WS_XNT, 1, 3);
}

__global__ __launch_bounds__(256, 2) void k2_main(char* __restrict__ ws) {
    __shared__ __attribute__((aligned(16))) char smem[K2_SMEM];
    const int b = blockIdx.x;
#define K2_MASK 3
    if (b < 256) { if (K2_MASK & 1) k2_gemm1(b, (const u16*)(ws + WS_XNF), (const u16*)(ws + WS_W1), (u16*)(ws + WS_HF), smem); }
    else if (K2_MASK & 2) k2_conv(b - 256, (const u16*)(ws + WS_XNT), (const u16*)(ws + WS_KR), (const float*)(ws + WS_KSC),
                 (u16*)(ws + WS_YT), smem);
}

constexpr int K3_RED = 4 * 3 * 4096 * 1;
constexpr int K3_YOFF = 64 * 1024;
constexpr int K3_YS = 72;
constexpr int K3_SMEM = K3_YOFF + 64 * K3_YS * 2;

__global__ __launch_bounds__(256, 1) void k3_main(const u16* __restrict__ x, const u16* __restrict__ dskip,
                                                  const char* __restrict__ ws, u16* __restrict__ out) {
    __shared__ __attribute__((aligned(16))) char smem[K3_SMEM];
    const int tid = threadIdx.x, lane = tid & 63;
    const int w = __builtin_amdgcn_readfirstlane(tid >> 6);
    const int bid = blockIdx.x, xcd = bid & 7, i5 = bid >> 3;
    const int lb = 4 * xcd + (i5 & 3), db = i5 >> 2;
    const u16* W2 = (const u16*)(ws + WS_W2);
    const u16* hF = (const u16*)(ws + WS_HF);
    const u16* yT = (const u16*)(ws + WS_YT);
    const u16* xnF = (const u16*)(ws + WS_XNF);
    const int r = lane & 15, q = lane >> 4;
    STAMP(ws, 2, 0);
    u32x4 yv[2];
#pragma unroll
    for (int i = 0; i < 2; ++i) {
        int c = tid + 256 * i;
        yv[i] = *(const u32x4*)(yT + (size_t)(64 * db + (c >> 3)) * L_ + 64 * lb + 8 * (c & 7));
    }
    const int lrow = 64 * lb + 16 * w + r;
    const int dcol = 64 * db + 16 * q;
    u32x4 xv[2], nv[2], dv[2];
#pragma unroll
    for (int i = 0; i < 2; ++i) {
        xv[i] = *(const u32x4*)(x + (size_t)lrow * D_ + dcol + 8 * i);
        int dd = dcol + 8 * i;
        nv[i] = *(const u32x4*)(xnF + xnf_off(lrow, dd >> 3));
        dv[i] = *(const u32x4*)(dskip + dd);
    }
    f32x4 acc[4][4];
#pragma unroll
    for (int t = 0; t < 4; ++t)
#pragma unroll
        for (int i = 0; i < 4; ++i) acc[t][i] = f32x4{0.f, 0.f, 0.f, 0.f};
    const u16* wp = W2 + ((size_t)(db * 56 + 14 * w) * 4) * 512 + lane * 8;
    const u16* hp = hF + ((size_t)(4 * lb) * 56 + 14 * w) * 512 + lane * 8;
    constexpr int PD = 4;
    bf16x8 wf[PD][4], hf[PD][4];
#pragma unroll
    for (int s = 0; s < PD; ++s) {
#pragma unroll
        for (int t = 0; t < 4; ++t) wf[s][t] = *(const bf16x8*)(wp + (s * 4 + t) * 512);
#pragma unroll
        for (int i = 0; i < 4; ++i) hf[s][i] = *(const bf16x8*)(hp + (i * 56 + s) * 512);
    }
    SCHED_FENCE();
#pragma unroll
    for (int k = 0; k < 14; ++k) {
        const int s = k % PD;
#pragma unroll
        for (int t = 0; t < 4; ++t)
#pragma unroll
            for (int i = 0; i < 4; ++i)
                acc[t][i] = __builtin_amdgcn_mfma_f32_16x16x32_bf16(wf[s][t], hf[s][i], acc[t][i], 0, 0, 0);
        if (k + PD < 14) {
#pragma unroll
            for (int t = 0; t < 4; ++t) wf[s][t] = *(const bf16x8*)(wp + ((k + PD) * 4 + t) * 512);
#pragma unroll
            for (int i = 0; i < 4; ++i) hf[s][i] = *(const bf16x8*)(hp + (i * 56 + k + PD) * 512);
        }
        SCHED_FENCE();
        if (k == 0) STAMP(ws, 2, 1);
    }
    STAMP(ws, 2, 2);
    {
        u16* yt = (u16*)(smem + K3_YOFF);
#pragma unroll
        for (int i = 0; i < 2; ++i) {
            int c = tid + 256 * i, dl = c >> 3, lp = 8 * (c & 7);
#pragma unroll
            for (int j = 0; j < 4; ++j) {
                yt[(lp + 2 * j) * K3_YS + dl] = (u16)(yv[i][j] & 0xffff);
                yt[(lp + 2 * j + 1) * K3_YS + dl] = (u16)(yv[i][j] >> 16);
            }
        }
    }
    {
        f32x4* red = (f32x4*)smem;
#pragma unroll
        for (int i = 0; i < 4; ++i) {
            if (i != w) {
#pragma unroll
                for (int t = 0; t < 4; ++t) red[((i * 4 + w) * 4 + t) * 64 + lane] = acc[t][i];
            }
        }
    }
    __syncthreads();
    f32x4 f[4];
    {
        const f32x4* red = (const f32x4*)smem;
#pragma unroll
        for (int t = 0; t < 4; ++t) {
            f32x4 own = w == 0 ? acc[t][0] : (w == 1 ? acc[t][1] : (w == 2 ? acc[t][2] : acc[t][3]));
            f[t] = own;
        }
#pragma unroll
        for (int sw = 0; sw < 4; ++sw) {
            if (sw != w) {
#pragma unroll
                for (int t = 0; t < 4; ++t) f[t] += red[((w * 4 + sw) * 4 + t) * 64 + lane];
            }
        }
    }
    const u16* yt = (const u16*)(smem + K3_YOFF) + (16 * w + r) * K3_YS + 16 * q;
    u32x4 yy[2];
    yy[0] = *(const u32x4*)(yt);
    yy[1] = *(const u32x4*)(yt + 8);
#pragma unroll
    for (int i = 0; i < 2; ++i) {
        u32x4 ov;
#pragma unroll
        for (int jj = 0; jj < 4; ++jj) {
            float res[2];
#pragma unroll
            for (int hh = 0; hh < 2; ++hh) {
                const int e = 8 * i + 2 * jj + hh;
                float fv = rbf(f[e >> 2][e & 3]);
                float g = rbf(fv * __builtin_amdgcn_rcpf(1.0f + __expf(-fv)));
                float xn = hh ? bfhi(nv[i][jj]) : bflo(nv[i][jj]);
                float ds = hh ? bfhi(dv[i][jj]) : bflo(dv[i][jj]);
                float yv1 = hh ? bfhi(yy[i][jj]) : bflo(yy[i][jj]);
                float xx = hh ? bfhi(xv[i][jj]) : bflo(xv[i][jj]);
                float s = rbf(yv1 + rbf(xn * ds));
                res[hh] = xx + rbf(s * g);
            }
            ov[jj] = pack_bf(res[0], res[1]);
        }
        *(u32x4*)(out + (size_t)lrow * D_ + dcol + 8 * i) = ov;
    }
    STAMP(ws, 2, 3);
}

extern "C" void kernel_launch(void* const* d_in, const int* in_sizes, int n_in,
                              void* d_out, int out_size, void* d_ws, size_t ws_size,
                              hipStream_t stream) {
    const u16* x   = (const u16*)d_in[0];
    const u16* lar = (const u16*)d_in[1];
    const u16* aim = (const u16*)d_in[2];
    const u16* cre = (const u16*)d_in[3];
    const u16* cim = (const u16*)d_in[4];
    const u16* ldt = (const u16*)d_in[5];
    const u16* dsk = (const u16*)d_in[6];
    const u16* wg  = (const u16*)d_in[8];
    const u16* wu  = (const u16*)d_in[9];
    const u16* wd  = (const u16*)d_in[10];
    char* ws = (char*)d_ws;
    k1_prep<<<K1_GRID, 256, 0, stream>>>(x, lar, aim, cre, cim, ldt, wg, wu, wd, ws);
    k2_main<<<512, 256, 0, stream>>>(ws);
    k3_main<<<256, 256, 0, stream>>>(x, dsk, ws, (u16*)d_out);
}
```

```cpp
#include <hip/hip_runtime.h>
#include <hip/hip_bf16.h>
#include <stdint.h>
#include <type_traits>

typedef __attribute__((ext_vector_type(8))) __bf16 bf16x8;
typedef __attribute__((ext_vector_type(2))) __bf16 bf16x2;
typedef __attribute__((ext_vector_type(8))) _Float16 f16x8;
typedef __attribute__((ext_vector_type(4))) float f32x4;
typedef __attribute__((ext_vector_type(2))) float f32x2;
typedef __attribute__((ext_vector_type(4))) unsigned int u32x4;
typedef __attribute__((ext_vector_type(2))) unsigned int u32x2;
typedef unsigned short u16;
#define GAS __attribute__((address_space(1)))
#define LAS __attribute__((address_space(3)))

constexpr int L_ = 2048, D_ = 512, NS_ = 128, F_ = 1792;

constexpr size_t WS_XNT  = 0;
constexpr size_t WS_STAT = 2u << 20;
constexpr size_t WS_FLAG = (2u << 20) + 65536;
constexpr size_t WS_W2   = 3u << 20;
constexpr size_t WS_HF   = 5u << 20;
constexpr size_t WS_YT   = 13u << 20;
constexpr size_t WS_KD   = 15u << 20;
constexpr size_t WS_KSC  = 17u << 20;

#define STAMP(ws_, kid, slot) do {} while (0)
#define SCHED_FENCE() __builtin_amdgcn_sched_barrier(0)

template <typename T> __device__ __forceinline__ void gstore(void* p, T v) {
    if constexpr (sizeof(T) == 16) asm volatile("global_store_dwordx4 %0, %1, off sc0 sc1\n\ts_nop 1" :: "v"(p), "v"(v) : "memory");
    else asm volatile("global_store_dwordx2 %0, %1, off sc0 sc1\n\ts_nop 1" :: "v"(p), "v"(v) : "memory");
}
__device__ __forceinline__ float bf2f(u16 v) { return __uint_as_float(((unsigned)v) << 16); }
__device__ __forceinline__ float bflo(unsigned v) { return __uint_as_float(v << 16); }
__device__ __forceinline__ float bfhi(unsigned v) { return __uint_as_float(v & 0xffff0000u); }
__device__ __forceinline__ u16 f2bf(float f) { __bf16 b = (__bf16)f; return __builtin_bit_cast(u16, b); }
__device__ __forceinline__ float rbf(float f) { return bf2f(f2bf(f)); }
__device__ __forceinline__ unsigned pack_bf(float lo, float hi) {
    return (unsigned)f2bf(lo) | ((unsigned)f2bf(hi) << 16);
}
__device__ __forceinline__ u16 f2h(float f) { _Float16 h = (_Float16)f; return __builtin_bit_cast(u16, h); }
typedef __attribute__((ext_vector_type(2))) _Float16 f16x2;
__device__ __forceinline__ unsigned pack_h(float lo, float hi) {
    return (unsigned)f2h(lo) | ((unsigned)f2h(hi) << 16);
}
__device__ __forceinline__ float wave_sum(float v) {
#pragma unroll
    for (int o = 32; o > 0; o >>= 1) v += __shfl_xor(v, o, 64);
    return v;
}
__device__ __forceinline__ float row16_sum(float v) {
    v += __builtin_bit_cast(float, __builtin_amdgcn_update_dpp(0, __builtin_bit_cast(int, v), 0x128, 0xf, 0xf, false));
    v += __builtin_bit_cast(float, __builtin_amdgcn_update_dpp(0, __builtin_bit_cast(int, v), 0x124, 0xf, 0xf, false));
    v += __builtin_bit_cast(float, __builtin_amdgcn_update_dpp(0, __builtin_bit_cast(int, v), 0x122, 0xf, 0xf, false));
    v += __builtin_bit_cast(float, __builtin_amdgcn_update_dpp(0, __builtin_bit_cast(int, v), 0x121, 0xf, 0xf, false));
    return v;
}
__device__ __forceinline__ float dot2bf(unsigned a, unsigned b, float c) {
    return __builtin_amdgcn_fdot2_f32_bf16(__builtin_bit_cast(bf16x2, a), __builtin_bit_cast(bf16x2, b), c, false);
}
__device__ __forceinline__ f32x2 ln_stats(float mean, float var) {
    const float mb = rbf(mean);
    const float sd = rbf(__builtin_sqrtf(rbf(rbf(var) + 1e-5f)));
    return f32x2{mb, 1.0f / sd};
}

#define SIDE_PRIO 1
constexpr int KA_G1 = 256, KA_LN = 64, KA_W2 = 56, KA_KG = 512;
constexpr int KA_GRID = KA_G1 + KA_LN + KA_W2 + KA_KG;
constexpr int G1_STAGE = 24 * 1024;
constexpr int G1_AUX = 3 * G1_STAGE;
constexpr int KA_SMEM = G1_AUX + 3072;

__device__ __forceinline__ float gelu_tanh(float x) {
    const float t = x * (-2.302208198f - 0.1029432397f * x * x);
    return x * __builtin_amdgcn_rcpf(1.0f + __builtin_amdgcn_exp2f(t));
}

__device__ __forceinline__ void glds16(unsigned ldsaddr, unsigned voff, const void* sbase) {
    asm volatile("s_mov_b32 m0, %0\n\ts_nop 0\n\tglobal_load_lds_dwordx4 %1, %2"
                 :: "s"(ldsaddr), "v"(voff), "s"(sbase) : "memory");
}
#define DS_RD128(dst, addr, off) asm volatile("ds_read_b128 %0, %1 offset:%2" : "=v"(dst) : "v"(addr), "i"(off))
#define DS_RDTR(dst, addr, off) asm volatile("ds_read_b64_tr_b16 %0, %1 offset:%2" : "=v"(dst) : "v"(addr), "i"(off))
typedef __attribute__((ext_vector_type(16))) float f32x16;

template <int NT>
__device__ __forceinline__ void ka_gemm1_wave(const int w, const int lane, const int mb, const int nb,
                                              const u16* __restrict__ x, const u16* __restrict__ wg,
                                              const u16* __restrict__ wu, u16* __restrict__ hF, char* smem, char* ws) {
    const int lg = w & 3;
    constexpr int fh = NT == 4 ? 0 : 1;
    constexpr int tl0 = NT == 4 ? 0 : 4;
    const unsigned lds0 = (unsigned)(uintptr_t)(LAS char*)smem;
    auto tile_of = [&](int t) { int v = t + lg; v = v >= NT ? v - NT : v; v = v >= NT ? v - NT : v; return tl0 + v; };
    const char* wbase = (const char*)(fh ? wu : wg) + 224 * nb;
    const char* xbase = (const char*)x + (size_t)(128 * mb + 16 * w) * 1024;
    unsigned wv[2], xv;
#pragma unroll
    for (int i = 0; i < 2; ++i) {
        const int r = 8 * lg + 4 * i + (lane >> 4), slot = lane & 15;
        const int sw = (r & 3) | (fh << 2);
        int g = (((slot >> 1) ^ sw) << 1) | (slot & 1);
        g = g > 13 ? 13 : g;
        wv[i] = r * (F_ * 2) + g * 16;
    }
    {
        const int lr = lane >> 2, pos = lane & 3;
        xv = lr * 1024 + ((pos ^ ((lr >> 2) & 3)) * 16);
    }
    const unsigned wdst = lds0 + fh * 8192 + lg * 2048, xdst = lds0 + 16384 + w * 1024;
    auto stage = [&](int ks, unsigned boff) {
        const char* wb = wbase + (size_t)ks * (32 * F_ * 2);
        glds16(wdst + boff, wv[0], wb);
        glds16(wdst + boff + 1024, wv[1], wb);
        glds16(xdst + boff, xv, xbase + ks * 64);
    };
    unsigned aw[NT], ax[2];
    {
        const int gu = (lane >> 4) & 1, h = lane >> 5, r = (lane >> 2) & 3, p = lane & 3;
        const int sw = r | (gu << 2);
#pragma unroll
        for (int t = 0; t < NT; ++t) aw[t] = lds0 + gu * 8192 + (8 * h + r) * 256 + ((tile_of(t) ^ sw) << 5) + 8 * p;
        const int lr = lane & 31;
#pragma unroll
        for (int s2 = 0; s2 < 2; ++s2) ax[s2] = lds0 + 16384 + (32 * lg + lr) * 64 + (((2 * s2 + h) ^ ((lr >> 2) & 3)) * 16);
    }
    f32x16 acc[NT];
#pragma unroll
    for (int t = 0; t < NT; ++t)
#pragma unroll
        for (int i = 0; i < 16; ++i) acc[t][i] = 0.f;
    u32x2 wl[2][NT], wh[2][NT];
    u32x4 xf[2];
    float cs = 0.f, sx = 0.f, sxx = 0.f;
    auto wfrag = [&](int s2, int t) { return u32x4{wl[s2][t][0], wl[s2][t][1], wh[s2][t][0], wh[s2][t][1]}; };
    stage(0, 0);
    stage(1, G1_STAGE);
    stage(2, 2 * G1_STAGE);
    asm volatile("s_waitcnt vmcnt(6)" ::: "memory");
    __builtin_amdgcn_s_barrier();
    SCHED_FENCE();
    STAMP(ws, 0, 1);
    DS_RD128(xf[0], ax[0], 0);
#pragma unroll
    for (int t = 0; t < NT; ++t) { DS_RDTR(wl[0][t], aw[t], 0); DS_RDTR(wh[0][t], aw[t], 1024); }
    DS_RD128(xf[1], ax[1], 0);
#pragma unroll
    for (int t = 0; t < NT; ++t) { DS_RDTR(wl[1][t], aw[t], 4096); DS_RDTR(wh[1][t], aw[t], 5120); }
    SCHED_FENCE();
    float cs2 = 0.f, sx2 = 0.f, sxx2 = 0.f;
    auto colsum = [&](u32x4 v) {
#pragma unroll
        for (int e = 0; e < 4; ++e) { cs += bflo(v[e]); cs2 += bfhi(v[e]); }
        asm volatile("" : "+v"(cs), "+v"(cs2));
    };
    auto rowstat = [&](u32x4 v) {
#pragma unroll
        for (int e = 0; e < 4; ++e) {
            const float lo = bflo(v[e]), hi = bfhi(v[e]);
            sx += lo; sxx = __builtin_fmaf(lo, lo, sxx); sx2 += hi; sxx2 = __builtin_fmaf(hi, hi, sxx2);
        }
        asm volatile("" : "+v"(sx), "+v"(sxx), "+v"(sx2), "+v"(sxx2));
    };
    auto iter = [&](int ks, auto bufc, auto modec) {
        constexpr int BUF = decltype(bufc)::value;
        constexpr int MODE = decltype(modec)::value;
        constexpr int NB = ((BUF + 1) % 3) * G1_STAGE;
        constexpr int PB = ((BUF + 2) % 3) * G1_STAGE;
#pragma unroll
        for (int t = 0; t < NT; ++t) {
            if (t == 0) asm volatile("s_waitcnt lgkmcnt(%5)" : "+v"(wl[0][0]), "+v"(wh[0][0]), "+v"(wl[0][1]), "+v"(wh[0][1]), "+v"(xf[0]) : "i"(4 * NT - 3));
            if (t == 2) {
                if constexpr (NT == 4) asm volatile("s_waitcnt lgkmcnt(%4)" : "+v"(wl[0][2]), "+v"(wh[0][2]), "+v"(wl[0][NT - 1]), "+v"(wh[0][NT - 1]) : "i"(2 * NT + 1));
                else asm volatile("s_waitcnt lgkmcnt(%2)" : "+v"(wl[0][2]), "+v"(wh[0][2]) : "i"(2 * NT + 1));
            }
            acc[t] = __builtin_amdgcn_mfma_f32_32x32x16_bf16(__builtin_bit_cast(bf16x8, wfrag(0, t)), __builtin_bit_cast(bf16x8, xf[0]), acc[t], 0, 0, 0);
            SCHED_FENCE();
            if (t == 0 && MODE == 0 && ks > 0) stage(ks + 2, PB);
            if (t == 1) colsum(wfrag(0, 0));
            if (t == 2 && fh == 0) rowstat(xf[0]);
            SCHED_FENCE();
        }
        if constexpr (NT == 4) asm volatile("s_waitcnt lgkmcnt(0)" : "+v"(xf[1]), "+v"(wl[1][0]), "+v"(wh[1][0]), "+v"(wl[1][1]), "+v"(wh[1][1]),
                                  "+v"(wl[1][2]), "+v"(wh[1][2]), "+v"(wl[1][NT - 1]), "+v"(wh[1][NT - 1]) :: "memory");
        else asm volatile("s_waitcnt lgkmcnt(0)" : "+v"(xf[1]), "+v"(wl[1][0]), "+v"(wh[1][0]), "+v"(wl[1][1]), "+v"(wh[1][1]),
                          "+v"(wl[1][2]), "+v"(wh[1][2]) :: "memory");
        if (MODE < 2) {
            if (MODE == 0) asm volatile("s_waitcnt vmcnt(3)" ::: "memory");
            else asm volatile("s_waitcnt vmcnt(0)" ::: "memory");
            SCHED_FENCE();
            __builtin_amdgcn_s_barrier();
        }
        SCHED_FENCE();
#pragma unroll
        for (int t = 0; t < NT; ++t) {
            acc[t] = __builtin_amdgcn_mfma_f32_32x32x16_bf16(__builtin_bit_cast(bf16x8, wfrag(1, t)), __builtin_bit_cast(bf16x8, xf[1]), acc[t], 0, 0, 0);
            SCHED_FENCE();
            if (MODE < 2) {
                if (t == 0) {
                    DS_RD128(xf[0], ax[0], NB);
#pragma unroll
                    for (int u = 0; u < 2; ++u) { DS_RDTR(wl[0][u], aw[u], NB); DS_RDTR(wh[0][u], aw[u], NB + 1024); }
                } else if (t == 1) {
#pragma unroll
                    for (int u = 2; u < NT; ++u) { DS_RDTR(wl[0][u], aw[u], NB); DS_RDTR(wh[0][u], aw[u], NB + 1024); }
                }
            }
            if (t == NT - 2) colsum(wfrag(1, 0));
            if (t == NT - 1 && fh == 1) rowstat(xf[1]);
            SCHED_FENCE();
        }
        if (MODE < 2) {
            DS_RD128(xf[1], ax[1], NB);
#pragma unroll
            for (int t = 0; t < NT; ++t) { DS_RDTR(wl[1][t], aw[t], NB + 4096); DS_RDTR(wh[1][t], aw[t], NB + 5120); }
        }
        SCHED_FENCE();
    };
    using I0 = std::integral_constant<int, 0>; using I1 = std::integral_constant<int, 1>; using I2 = std::integral_constant<int, 2>;
#pragma unroll 1
    for (int k3 = 0; k3 < 12; k3 += 3) {
        iter(k3, I0{}, I0{});
        iter(k3 + 1, I1{}, I0{});
        iter(k3 + 2, I2{}, I0{});
    }
    iter(12, I0{}, I0{});
    iter(13, I1{}, I0{});
    iter(14, I2{}, I1{});
    iter(15, I0{}, I2{});
    STAMP(ws, 0, 2);
    f32x2* stp = (f32x2*)(smem + G1_AUX);
    float* cst = (float*)(smem + G1_AUX + 2048);
    sx += sx2; sxx += sxx2; cs += cs2;
    sx += __shfl_xor(sx, 32, 64); sxx += __shfl_xor(sxx, 32, 64); cs += __shfl_xor(cs, 32, 64);
    if (lane < 32) {
        stp[fh * 128 + 32 * lg + lane] = f32x2{sx, sxx};
        cst[tile_of(0) * 32 + lane] = cs;
    }
    __syncthreads();
    f32x2 st;
    {
        const f32x2 p0 = stp[32 * lg + (lane & 31)], p1 = stp[128 + 32 * lg + (lane & 31)];
        float mean = (p0[0] + p1[0]) * (1.0f / D_);
        float var = fmaxf((p0[1] + p1[1]) * (1.0f / D_) - mean * mean, 0.f);
        if (__any(mean * mean > 32.0f * var)) {
            for (int r = 0; r < 32; ++r) {
                const u32x4 raw = *(const u32x4*)(x + (size_t)(128 * mb + 32 * lg + r) * D_ + lane * 8);
                float v[8], s = 0.f, q2 = 0.f;
#pragma unroll
                for (int e = 0; e < 4; ++e) { v[2 * e] = bflo(raw[e]); v[2 * e + 1] = bfhi(raw[e]); }
#pragma unroll
                for (int e = 0; e < 8; ++e) s += v[e];
                s = wave_sum(s) * (1.0f / D_);
#pragma unroll
                for (int e = 0; e < 8; ++e) q2 += (v[e] - s) * (v[e] - s);
                q2 = wave_sum(q2) * (1.0f / D_);
                if ((lane & 31) == r) { mean = s; var = q2; }
            }
        }
        st = ln_stats(mean, var);
    }
    const int hh = lane >> 5;
    const int lt = 8 * mb + 2 * lg + ((lane >> 4) & 1);
#pragma unroll
    for (int t = 0; t < NT; ++t) {
        const int T = 7 * nb + tile_of(t);
        const float* cp = cst + tile_of(t) * 32 + 4 * hh;
        u32x4 p;
#pragma unroll
        for (int i2 = 0; i2 < 2; ++i2) {
            const f32x4 cg = *(const f32x4*)(cp + 8 * i2), cu = *(const f32x4*)(cp + 16 + 8 * i2);
            float hv[4];
#pragma unroll
            for (int r = 0; r < 4; ++r) {
                const float g = rbf((acc[t][4 * i2 + r] - st[0] * cg[r]) * st[1]);
                const float u = rbf((acc[t][8 + 4 * i2 + r] - st[0] * cu[r]) * st[1]);
                hv[r] = rbf(gelu_tanh(g)) * u;
            }
            p[2 * i2] = pack_bf(hv[0], hv[1]);
            p[2 * i2 + 1] = pack_bf(hv[2], hv[3]);
        }
        gstore((char*)hF + ((size_t)(lt * 56 + (T >> 1)) * 64 + (2 * (T & 1) + hh) * 16 + (lane & 15)) * 16, p);
    }
}

__device__ __forceinline__ void ka_ln(int b, const u16* __restrict__ x, u16* __restrict__ xnT, f32x2* __restrict__ stat,
                                      char* smem) {
    const int tid = threadIdx.x, w = tid >> 6, lane = tid & 63, rr = lane >> 4, c = lane & 15;
    constexpr int TS = 516;
    u16* tile = (u16*)smem;
    const int l0 = b * 32, rl = 4 * w + rr, row = l0 + rl;
    u32x4 raw[4];
#pragma unroll
    for (int i = 0; i < 4; ++i) raw[i] = *(const u32x4*)(x + (size_t)row * D_ + (c + 16 * i) * 8);
    float v[32];
#pragma unroll
    for (int i = 0; i < 4; ++i)
#pragma unroll
        for (int j = 0; j < 4; ++j) { v[8 * i + 2 * j] = bflo(raw[i][j]); v[8 * i + 2 * j + 1] = bfhi(raw[i][j]); }
    float s = 0.f;
#pragma unroll
    for (int j = 0; j < 32; ++j) s += v[j];
    const float mean = row16_sum(s) * (1.0f / D_);
    float q2 = 0.f;
#pragma unroll
    for (int j = 0; j < 32; ++j) { float dlt = v[j] - mean; q2 += dlt * dlt; }
    const f32x2 st = ln_stats(mean, row16_sum(q2) * (1.0f / D_));
    if (c == 0) gstore(stat + row, st);
#pragma unroll
    for (int i = 0; i < 4; ++i) {
        u32x4 ph;
#pragma unroll
        for (int j = 0; j < 4; ++j)
            ph[j] = pack_h(rbf(rbf(v[8 * i + 2 * j] - st[0]) * st[1]), rbf(rbf(v[8 * i + 2 * j + 1] - st[0]) * st[1]));
        u16* tp = tile + rl * TS + (c + 16 * i) * 8;
        *(u32x2*)(tp) = u32x2{ph[0], ph[1]};
        *(u32x2*)(tp + 4) = u32x2{ph[2], ph[3]};
    }
    __syncthreads();
#pragma unroll
    for (int it = 0; it < 4; ++it) {
        int d = it * 128 + w * 16 + (lane >> 2), part = lane & 3;
        const u16* sp = tile + (8 * part) * TS + d;
        u32x4 o;
#pragma unroll
        for (int j = 0; j < 4; ++j) o[j] = (unsigned)sp[(2 * j) * TS] | ((unsigned)sp[(2 * j + 1) * TS] << 16);
        gstore(xnT + (size_t)d * L_ + l0 + 8 * part, o);
    }
}

__device__ __forceinline__ void ka_w2(int ks, const u16* __restrict__ wd, u16* __restrict__ W2, char* smem) {
    const int tid = threadIdx.x;
    u16* tile = (u16*)smem;
    u32x4 v[4];
#pragma unroll
    for (int i = 0; i < 4; ++i) {
        int c = tid + 512 * i, r = c >> 6, ch = c & 63;
        v[i] = *(const u32x4*)(wd + (size_t)(32 * ks + r) * D_ + ch * 8);
    }
    SCHED_FENCE();
#pragma unroll
    for (int i = 0; i < 4; ++i) {
        int c = tid + 512 * i, r = c >> 6, ch = c & 63;
        *(u32x4*)(tile + r * 520 + ch * 8) = v[i];
    }
    __syncthreads();
#pragma unroll
    for (int i = 0; i < 4; ++i) {
        int db = 2 * i + (tid >> 8), t = (tid >> 6) & 3, lane = tid & 63, r = lane & 15, q = lane >> 4;
        int dl = 64 * db + 16 * (r >> 2) + 4 * t + (r & 3);
        const u16* sp = tile + (16 * (q >> 1) + 4 * (q & 1)) * 520 + dl;
        u32x4 o4;
        o4[0] = (unsigned)sp[0 * 520] | ((unsigned)sp[1 * 520] << 16);
        o4[1] = (unsigned)sp[2 * 520] | ((unsigned)sp[3 * 520] << 16);
        o4[2] = (unsigned)sp[8 * 520] | ((unsigned)sp[9 * 520] << 16);
        o4[3] = (unsigned)sp[10 * 520] | ((unsigned)sp[11 * 520] << 16);
        gstore(W2 + ((size_t)(db * 56 + ks) * 4 + t) * 512 + lane * 8, o4);
    }
}

__device__ __forceinline__ int bset_idx(int e) {
    if (e < 16) return e;
    int s = (31 - __builtin_clz(e)) - 3;
    return 8 + 8 * s + ((e - (8 << s)) >> s);
}
struct cplx { float r, i; };
__device__ __forceinline__ cplx cmul(cplx a, cplx b) { return cplx{a.r * b.r - a.i * b.i, a.r * b.i + a.i * b.r}; }
__device__ __forceinline__ cplx cexp_p(float zr2, float zi2, float p) {
    float mag = __builtin_amdgcn_exp2f(zr2 * p);
    float ph = __builtin_amdgcn_fractf(zi2 * p);
    return cplx{mag * __builtin_amdgcn_cosf(ph), mag * __builtin_amdgcn_sinf(ph)};
}

constexpr int CV_RS = 528;
__device__ __forceinline__ void ka_kgen(const int d, const u16* __restrict__ lar, const u16* __restrict__ aim,
                                        const u16* __restrict__ cre, const u16* __restrict__ cim,
                                        const u16* __restrict__ ldt, char* __restrict__ ws, char* smem) {
    if (threadIdx.x >= 256) return;
    const int tid = threadIdx.x, lane = tid & 63;
    const int w = __builtin_amdgcn_readfirstlane(tid >> 6);
    char* Ut = smem;
    char* Vt = smem + 41 * CV_RS;
    const int n = (w & 1) * 64 + lane;
    const u16 r_ldt = ldt[d];
    const u16 r_c0 = cre[d * NS_ + lane], r_c1 = cim[d * NS_ + lane], r_c2 = cre[d * NS_ + 64 + lane], r_c3 = cim[d * NS_ + 64 + lane];
    const u16 r_lar = lar[d * NS_ + n], r_aim = aim[d * NS_ + n];
    SCHED_FENCE();
    const float dt = rbf(__expf(bf2f(r_ldt)));
    const float sabs = wave_sum(fabsf(bf2f(r_c0)) + fabsf(bf2f(r_c1)) + fabsf(bf2f(r_c2)) + fabsf(bf2f(r_c3))) * dt;
    int ex = (int)((__float_as_uint(sabs) >> 23) & 0xff);
    int se = 127 + 14 - (ex - 126);
    se = se < 1 ? 1 : (se > 254 ? 254 : se);
    const float sc = __uint_as_float((unsigned)se << 23);
    {
        const float ar = -rbf(__expf(bf2f(r_lar)));
        const float zr = ar * dt, zi = bf2f(r_aim) * dt;
        const bool isU = w >= 2;
        const float pb = isU ? 16.0f : 1.0f;
        const float zr2 = zr * 1.4426950408889634f;
        const float zi2 = zi * (isU ? -0.15915494309189535f : 0.15915494309189535f);
        cplx e[8];
#pragma unroll
        for (int k = 0; k < 8; ++k) e[k] = cexp_p(zr2, zi2, pb * (float)(1 << k));
        cplx v[41];
        {
            const float crv = bf2f((w & 1) ? r_c2 : r_c0), civ = bf2f((w & 1) ? r_c3 : r_c1);
            v[0] = isU ? cplx{crv * dt * sc, -(civ * dt * sc)} : cplx{1.0f, 0.0f};
        }
        char* tb = (isU ? Ut : Vt) + n * 4;
        auto put = [&](int i0, int i1) {
#pragma unroll
            for (int i = i0; i < i1; ++i) *(unsigned*)(tb + i * CV_RS) = pack_h(v[i].r, v[i].i);
            SCHED_FENCE();
        };
        v[1] = cmul(v[0], e[0]);
#pragma unroll
        for (int k = 0; k < 2; ++k) v[2 + k] = cmul(v[k], e[1]);
#pragma unroll
        for (int k = 0; k < 4; ++k) v[4 + k] = cmul(v[k], e[2]);
#pragma unroll
        for (int k = 0; k < 8; ++k) v[8 + k] = cmul(v[k], e[3]);
        v[40] = cmul(v[0], e[7]);
        if (isU) *(unsigned*)(tb + 40 * CV_RS) = pack_h(v[40].r, v[40].i);
        put(0, 16);
#pragma unroll
        for (int k = 0; k < 8; ++k) v[16 + k] = cmul(v[2 * k], e[4]);
        put(16, 24);
#pragma unroll
        for (int k = 0; k < 8; ++k) v[24 + k] = cmul(v[k < 4 ? 4 * k : 16 + (4 * k - 16) / 2], e[5]);
        put(24, 32);
#pragma unroll
        for (int k = 0; k < 8; ++k) {
            const int ee = 8 * k;
            const int src = ee < 16 ? ee : (ee < 32 ? 16 + (ee - 16) / 2 : 24 + (ee - 32) / 4);
            v[32 + k] = cmul(v[src], e[6]);
        }
        put(32, 40);
    }
    __syncthreads();
    const int j = lane & 15, q = lane >> 4;
    {
        const int urow = (w == 0) ? j : (8 + 8 * w + j);
        const int vrow = bset_idx(j << w);
        const char* ua = Ut + urow * CV_RS + q * 16;
        const char* vb = Vt + vrow * CV_RS + q * 16;
        f16x8 a[8], bb[8];
#pragma unroll
        for (int ks = 0; ks < 8; ++ks) { a[ks] = *(const f16x8*)(ua + ks * 64); bb[ks] = *(const f16x8*)(vb + ks * 64); }
        SCHED_FENCE();
        f32x4 acc0 = {0.f, 0.f, 0.f, 0.f}, acc1 = {0.f, 0.f, 0.f, 0.f};
#pragma unroll
        for (int ks = 0; ks < 8; ks += 2) {
            acc0 = __builtin_amdgcn_mfma_f32_16x16x32_f16(a[ks], bb[ks], acc0, 0, 0, 0);
            acc1 = __builtin_amdgcn_mfma_f32_16x16x32_f16(a[ks + 1], bb[ks + 1], acc1, 0, 0, 0);
        }
        u32x2 o = {pack_h(acc0[0] + acc1[0], acc0[1] + acc1[1]), pack_h(acc0[2] + acc1[2], acc0[3] + acc1[3])};
        gstore((u16*)(ws + WS_KD) + (size_t)d * 1024 + w * 256 + j * 16 + 4 * q, o);
        if (tid == 0) ((float*)(ws + WS_KSC))[d] = 2.0f / sc;
    }
}

__global__ __launch_bounds__(512, 4) void ka_main(const u16* __restrict__ x, const u16* __restrict__ wg,
                                                  const u16* __restrict__ wu, const u16* __restrict__ wd,
                                                  const u16* __restrict__ lar, const u16* __restrict__ aim,
                                                  const u16* __restrict__ cre, const u16* __restrict__ cim,
                                                  const u16* __restrict__ ldt, char* __restrict__ ws) {
    __shared__ __attribute__((aligned(16))) char smem[KA_SMEM];
    int b = blockIdx.x;
    STAMP(ws, 0, 0);
    if (b < KA_G1) {
        const int lane = threadIdx.x & 63;
        const int w = __builtin_amdgcn_readfirstlane(threadIdx.x >> 6);
        const int xcd = b & 7, i5 = b >> 3;
        const int mb = (xcd & 1) * 8 + (i5 & 7), nb = (xcd >> 1) * 4 + (i5 >> 3);
        if (w < 4) ka_gemm1_wave<4>(w, lane, mb, nb, x, wg, wu, (u16*)(ws + WS_HF), smem, ws);
        else ka_gemm1_wave<3>(w, lane, mb, nb, x, wg, wu, (u16*)(ws + WS_HF), smem, ws);
    } else if (b < KA_G1 + KA_LN) {
        __builtin_amdgcn_s_setprio(SIDE_PRIO);
        ka_ln(b - KA_G1, x, (u16*)(ws + WS_XNT), (f32x2*)(ws + WS_STAT), smem);
    } else if (b < KA_G1 + KA_LN + KA_W2) {
        b -= KA_G1 + KA_LN;
        __builtin_amdgcn_s_setprio(SIDE_PRIO);
        if (b == 0) ((unsigned*)(ws + WS_FLAG))[threadIdx.x] = 0u;
        ka_w2(b, wd, (u16*)(ws + WS_W2), smem);
    } else {
        __builtin_amdgcn_s_setprio(SIDE_PRIO);
        ka_kgen(b - (KA_G1 + KA_LN + KA_W2), lar, aim, cre, cim, ldt, ws, smem);
    }
    STAMP(ws, 0, 3);
}

constexpr int KB_CV = 512, KB_G2 = 256;
constexpr int CV_KD = 40960;
constexpr int CV_COPY = 4160;
constexpr int CV_XOFF = 8 * CV_COPY;
constexpr int KB_SMEM = 49152;

struct IdxTab { u16 v[2064]; };
constexpr IdxTab make_idx_tab() {
    IdxTab t{};
    for (int i = 0; i < 2064; ++i) {
        int lag = 2047 - i;
        if (lag < 0) { t.v[i] = 1024; continue; }
        int p = lag;
        if (lag >= 256) {
            int s = 0;
            while ((lag >> s) >= 256) ++s;
            int step = 1 << s, rem = lag & (step - 1), base = lag - rem, half = step >> 1;
            if (rem > half || (rem == half && ((base >> s) & 1))) base += step;
            p = base;
        }
        int e = 0, r = p;
        if (p >= 256) {
            while ((p >> e) >= 256) ++e;
            if (e > 3) e = 3;
            r = (p - (128 << e)) >> e;
        }
        t.v[i] = (u16)(e * 256 + (r & 15) * 16 + (r >> 4));
    }
    return t;
}
__device__ const IdxTab kIdxTab = make_idx_tab();

__device__ __forceinline__ void kb_conv(const int d, char* __restrict__ ws, char* smem) {
    const int tid = threadIdx.x, lane = tid & 63;
    const int w = __builtin_amdgcn_readfirstlane(tid >> 6);
    __builtin_amdgcn_s_setprio(2);
    u16* Kd = (u16*)(smem + CV_KD);
    const u32x2 kdv = *(const u32x2*)((const u16*)(ws + WS_KD) + (size_t)d * 1024 + tid * 4);
    const float osc = ((const float*)(ws + WS_KSC))[d];
    const u32x4 it0 = *(const u32x4*)(kIdxTab.v + 8 * tid), it1 = *(const u32x4*)(kIdxTab.v + 8 * tid + 8);
    const u32x4 xrow = *(const u32x4*)((const u16*)(ws + WS_XNT) + (size_t)d * L_ + tid * 8);
    SCHED_FENCE();
    *(u32x2*)(Kd + tid * 4) = kdv;
    if (tid == 0) Kd[1024] = 0;
    __syncthreads();
    STAMP(ws, 1, 1);
    const int j = lane & 15, q = lane >> 4;
    {
        unsigned wd[8];
#pragma unroll
        for (int e = 0; e < 4; ++e) {
            wd[e] = (unsigned)Kd[it0[e] & 0xffff] | ((unsigned)Kd[it0[e] >> 16] << 16);
            wd[4 + e] = (unsigned)Kd[it1[e] & 0xffff] | ((unsigned)Kd[it1[e] >> 16] << 16);
        }
        char* dst = smem + tid * 16;
#pragma unroll
        for (int e = 0; e < 4; ++e) {
            u32x4 ev = {wd[e], wd[e + 1], wd[e + 2], wd[e + 3]};
            *(u32x4*)(dst + (2 * e) * CV_COPY) = ev;
            u32x4 od;
#pragma unroll
            for (int k = 0; k < 4; ++k) od[k] = __builtin_amdgcn_alignbit(wd[e + k + 1], wd[e + k], 16);
            *(u32x4*)(dst + (2 * e + 1) * CV_COPY) = od;
        }
        char* xp = smem + CV_XOFF;
        if (tid < 32) *(u32x4*)(xp + tid * 16) = u32x4{0u, 0u, 0u, 0u};
        if (tid >= 64 && tid < 72) *(u32x4*)(smem + (tid - 64) * CV_COPY + 256 * 16) = u32x4{0u, 0u, 0u, 0u};
        *(u32x4*)(xp + 512 + tid * 16) = xrow;
    }
    __syncthreads();
    STAMP(ws, 1, 2);
    const char* kb = smem + ((7 - j) & 7) * CV_COPY + (253 + q - (j >> 3)) * 16 - w * 2 * 64;
    const char* xq = smem + CV_XOFF + (256 + 16 * j - 16 + 8 * q) * 2 - w * 2 * 64;
    f32x4 acc[8];
#pragma unroll
    for (int i = 0; i < 8; ++i) acc[i] = f32x4{0.f, 0.f, 0.f, 0.f};
    {
        f16x8 bfr[2][8], af[3];
#pragma unroll
        for (int c = 0; c < 8; ++c) bfr[0][c] = *(const f16x8*)(xq + c * 512);
        af[0] = *(const f16x8*)(kb);
        af[1] = *(const f16x8*)(kb - 8 * 64);
        SCHED_FENCE();
#pragma unroll
        for (int ri = 0; ri < 2; ++ri) {
#pragma unroll
            for (int mm = 0; mm < 8; ++mm) {
                const int g = ri * 8 + mm;
                if (g + 2 < 16) {
                    const int g2 = g + 2;
                    af[g2 % 3] = *(const f16x8*)(kb - (8 * (g2 & 7) + (g2 >> 3)) * 64);
                }
                if (ri < 1) bfr[1][mm] = *(const f16x8*)(xq + mm * 512 - 64);
#pragma unroll
                for (int ct = mm; ct < 8; ++ct)
                    acc[ct] = __builtin_amdgcn_mfma_f32_16x16x32_f16(af[g % 3], bfr[ri][ct - mm], acc[ct], 0, 0, 0);
                SCHED_FENCE();
            }
        }
    }
    STAMP(ws, 1, 4);
    __syncthreads();
    {
        f32x4* exb = (f32x4*)(smem + w * 8192) + lane;
#pragma unroll
        for (int i = 0; i < 8; ++i) exb[i * 64] = acc[i];
    }
    __syncthreads();
    {
        u16* yp = (u16*)(ws + WS_YT) + (size_t)d * L_ + 16 * j + 4 * q + 512 * w;
#pragma unroll
        for (int i = 0; i < 2; ++i) {
            const f32x4* exb = (const f32x4*)(smem + (2 * w + i) * 1024) + lane;
            f32x4 o = exb[0] + exb[512] + exb[1024] + exb[1536];
            u32x2 p = {pack_bf(o[0] * osc, o[1] * osc), pack_bf(o[2] * osc, o[3] * osc)};
            gstore(yp + 256 * i, p);
        }
    }
    STAMP(ws, 1, 6);
    asm volatile("s_waitcnt vmcnt(0)" ::: "memory");
    __syncthreads();
    if (tid == 0) __hip_atomic_fetch_add((GAS unsigned*)(ws + WS_FLAG) + 64 * (d >> 6), 1u, __ATOMIC_RELAXED, __HIP_MEMORY_SCOPE_AGENT);
}

__device__ __forceinline__ void kb_gemm2(const int bid, const u16* __restrict__ x, const u16* __restrict__ dskip,
                                         char* __restrict__ ws, u16* __restrict__ out, char* smem) {
    const int tid = threadIdx.x, lane = tid & 63;
    const int w = __builtin_amdgcn_readfirstlane(tid >> 6);
    const int xcd = bid & 7, i5 = bid >> 3;
    const int lb = 4 * xcd + (i5 & 3), db = i5 >> 2;
    const int r = lane & 15, q = lane >> 4;
    const int lrow = 64 * lb + 16 * w + r;
    const int dcol = 64 * db + 16 * q;
    f32x4 acc[4][4];
#pragma unroll
    for (int t = 0; t < 4; ++t)
#pragma unroll
        for (int i = 0; i < 4; ++i) acc[t][i] = f32x4{0.f, 0.f, 0.f, 0.f};
    const __amdgpu_buffer_rsrc_t wrs = __builtin_amdgcn_make_buffer_rsrc(ws + WS_W2, 0, F_ * D_ * 2, 0x00020000);
    const __amdgpu_buffer_rsrc_t hrs = __builtin_amdgcn_make_buffer_rsrc(ws + WS_HF, 0, L_ * F_ * 2, 0x00020000);
    const int wso = (db * 56 + 14 * w) * 4096, hso = (4 * lb * 56 + 14 * w) * 1024;
    const int l16 = lane * 16;
    auto ldw = [&](int k, int t) { return __builtin_bit_cast(bf16x8, __builtin_amdgcn_raw_buffer_load_b128(wrs, l16, wso + (k * 4 + t) * 1024, 0)); };
    auto ldh = [&](int k, int i) { return __builtin_bit_cast(bf16x8, __builtin_amdgcn_raw_buffer_load_b128(hrs, l16, hso + (i * 56 + k) * 1024, 0)); };
#define G2_PD 2
    constexpr int PD = G2_PD;
    bf16x8 wf[PD][4], hf[PD][4];
#pragma unroll
    for (int s = 0; s < PD; ++s) {
#pragma unroll
        for (int t = 0; t < 4; ++t) wf[s][t] = ldw(s, t);
#pragma unroll
        for (int i = 0; i < 4; ++i) hf[s][i] = ldh(s, i);
    }
    SCHED_FENCE();
    const __amdgpu_buffer_rsrc_t yrs = __builtin_amdgcn_make_buffer_rsrc(ws + WS_YT, 0, D_ * L_ * 2, 0x00020000);
    auto poll = [&]() {
        return __hip_atomic_load((GAS unsigned*)(ws + WS_FLAG) + 64 * db, __ATOMIC_RELAXED, __HIP_MEMORY_SCOPE_AGENT);
    };
    auto ldy = [&](u32x4 (&yv)[2]) {
        int ln = l16; asm volatile("" : "+v"(ln));
        const int yo = (ln >> 5) * (L_ * 2) + (ln & 16);
        const int so = (64 * db * L_ + 64 * lb + 16 * w) * 2;
        yv[0] = __builtin_amdgcn_raw_buffer_load_b128(yrs, yo, so, 16);
        yv[1] = __builtin_amdgcn_raw_buffer_load_b128(yrs, yo, so + 32 * L_ * 2, 16);
    };
    unsigned fv = 0;
    bool early = false;
    u32x4 yv[2];
    u32x4 xv[2], dv[2];
    f32x2 st;
#pragma unroll
    for (int k = 0; k < 14; ++k) {
        const int s = k % PD;
#pragma unroll
        for (int t = 0; t < 4; ++t) {
#pragma unroll
            for (int i = 0; i < 4; ++i)
                acc[t][i] = __builtin_amdgcn_mfma_f32_16x16x32_bf16(wf[s][t], hf[s][i], acc[t][i], 0, 0, 0);
            SCHED_FENCE();
            if (k + PD < 14) wf[s][t] = ldw(k + PD, t);
            SCHED_FENCE();
        }
        if (k + PD < 14) {
#pragma unroll
            for (int i = 0; i < 4; ++i) hf[s][i] = ldh(k + PD, i);
        }
        if (k == 10) fv = poll();
        if (k == 12) {
            early = __builtin_amdgcn_readfirstlane(fv) == 64u;
            if (early) ldy(yv);
        }
        if (k == 13) {
#pragma unroll
            for (int i = 0; i < 2; ++i) {
                xv[i] = *(const u32x4*)(x + (size_t)lrow * D_ + dcol + 8 * i);
                dv[i] = *(const u32x4*)(dskip + dcol + 8 * i);
            }
            st = ((const f32x2*)(ws + WS_STAT))[lrow];
        }
        SCHED_FENCE();
        if (k == 0) STAMP(ws, 1, 1);
    }
    STAMP(ws, 1, 2);
    {
        f32x4* red = (f32x4*)smem;
#pragma unroll
        for (int i = 0; i < 4; ++i) {
            if (i != w) {
                const int si = w < i ? w : w - 1;
#pragma unroll
                for (int t = 0; t < 4; ++t) red[((i * 3 + si) * 4 + t) * 64 + lane] = acc[t][i];
            }
        }
    }
    if (w == 0 && !early) {
        for (unsigned spins = 0; spins < (1u << 22); ++spins) {
            if (__builtin_amdgcn_readfirstlane(poll()) == 64u) break;
            __builtin_amdgcn_s_sleep(8);
        }
    }
    __syncthreads();
    if (!early) ldy(yv);
    STAMP(ws, 1, 4);
    f32x4 f[4];
    {
        const f32x4* red = (const f32x4*)smem;
#pragma unroll
        for (int t = 0; t < 4; ++t) f[t] = w == 0 ? acc[t][0] : (w == 1 ? acc[t][1] : (w == 2 ? acc[t][2] : acc[t][3]));
#pragma unroll
        for (int si = 0; si < 3; ++si)
#pragma unroll
            for (int t = 0; t < 4; ++t) f[t] += red[((w * 3 + si) * 4 + t) * 64 + lane];
    }
    u16 yt[16];
    {
        char* yb = smem + w * 12288;
        asm volatile("" ::: "memory");
        *(u32x4*)(yb + lane * 16) = yv[0];
        *(u32x4*)(yb + 1024 + lane * 16) = yv[1];
        asm volatile("s_waitcnt lgkmcnt(0)" ::: "memory");
        const u16* yr = (const u16*)(yb + q * 512 + 2 * r);
#pragma unroll
        for (int e = 0; e < 16; ++e) yt[e] = yr[e * 16];
    }
#pragma unroll
    for (int i = 0; i < 2; ++i) {
        u32x4 ov;
#pragma unroll
        for (int jj = 0; jj < 4; ++jj) {
            float res[2];
#pragma unroll
            for (int hh = 0; hh < 2; ++hh) {
                const int e = 8 * i + 2 * jj + hh;
                const float fv = rbf(f[e >> 2][e & 3]);
                const float g = rbf(fv * __builtin_amdgcn_rcpf(1.0f + __expf(-fv)));
                const float ds = hh ? bfhi(dv[i][jj]) : bflo(dv[i][jj]);
                const float xx = hh ? bfhi(xv[i][jj]) : bflo(xv[i][jj]);
                const float xn = rbf(rbf(xx - st[0]) * st[1]);
                const float s = rbf(bf2f(yt[e]) + rbf(xn * ds));
                res[hh] = xx + rbf(s * g);
            }
            ov[jj] = pack_bf(res[0], res[1]);
        }
        *(u32x4*)(out + (size_t)lrow * D_ + dcol + 8 * i) = ov;
    }
}

__global__ __launch_bounds__(256, 3) void kb_main(const u16* __restrict__ x,
                                                  const u16* __restrict__ dskip, char* __restrict__ ws,
                                                  u16* __restrict__ out) {
    __shared__ __attribute__((aligned(16))) char smem[KB_SMEM];
    const int b = blockIdx.x;
    STAMP(ws, 1, 0);
    if (b < KB_CV) kb_conv(b, ws, smem);
    else kb_gemm2(b - KB_CV, x, dskip, ws, out, smem);
    STAMP(ws, 1, 3);
}

extern "C" void kernel_launch(void* const* d_in, const int* in_sizes, int n_in,
                              void* d_out, int out_size, void* d_ws, size_t ws_size,
                              hipStream_t stream) {
    const u16* x   = (const u16*)d_in[0];
    const u16* lar = (const u16*)d_in[1];
    const u16* aim = (const u16*)d_in[2];
    const u16* cre = (const u16*)d_in[3];
    const u16* cim = (const u16*)d_in[4];
    const u16* ldt = (const u16*)d_in[5];
    const u16* dsk = (const u16*)d_in[6];
    const u16* wg  = (const u16*)d_in[8];
    const u16* wu  = (const u16*)d_in[9];
    const u16* wd  = (const u16*)d_in[10];
    char* ws = (char*)d_ws;
    ka_main<<<KA_GRID, 512, 0, stream>>>(x, wg, wu, wd, lar, aim, cre, cim, ldt, ws);
    kb_main<<<KB_CV + KB_G2, 256, 0, stream>>>(x, dsk, ws, (u16*)d_out);
}
```

```cpp
#include <hip/hip_runtime.h>
#include <hip/hip_bf16.h>
#include <stdint.h>
#include <type_traits>

typedef __attribute__((ext_vector_type(8))) __bf16 bf16x8;
typedef __attribute__((ext_vector_type(2))) __bf16 bf16x2;
typedef __attribute__((ext_vector_type(8))) _Float16 f16x8;
typedef __attribute__((ext_vector_type(4))) float f32x4;
typedef __attribute__((ext_vector_type(2))) float f32x2;
typedef __attribute__((ext_vector_type(4))) unsigned int u32x4;
typedef __attribute__((ext_vector_type(2))) unsigned int u32x2;
typedef unsigned short u16;
#define GAS __attribute__((address_space(1)))
#define LAS __attribute__((address_space(3)))

constexpr int L_ = 2048, D_ = 512, NS_ = 128, F_ = 1792;

constexpr size_t WS_XNT  = 0;
constexpr size_t WS_STAT = 2u << 20;
constexpr size_t WS_FLAG = (2u << 20) + 65536;
constexpr size_t WS_W2   = 3u << 20;
constexpr size_t WS_HF   = 5u << 20;
constexpr size_t WS_YT   = 13u << 20;
constexpr size_t WS_KD   = 15u << 20;
constexpr size_t WS_KSC  = 17u << 20;

#define STAMP(ws_, kid, slot) do {} while (0)
#define SCHED_FENCE() __builtin_amdgcn_sched_barrier(0)

template <typename T> __device__ __forceinline__ void gstore(void* p, T v) {
    if constexpr (sizeof(T) == 16) asm volatile("global_store_dwordx4 %0, %1, off sc0 sc1\n\ts_nop 1" :: "v"(p), "v"(v));
    else asm volatile("global_store_dwordx2 %0, %1, off sc0 sc1\n\ts_nop 1" :: "v"(p), "v"(v));
}
__device__ __forceinline__ float bf2f(u16 v) { return __uint_as_float(((unsigned)v) << 16); }
__device__ __forceinline__ float bflo(unsigned v) { return __uint_as_float(v << 16); }
__device__ __forceinline__ float bfhi(unsigned v) { return __uint_as_float(v & 0xffff0000u); }
__device__ __forceinline__ u16 f2bf(float f) { __bf16 b = (__bf16)f; return __builtin_bit_cast(u16, b); }
__device__ __forceinline__ float rbf(float f) { return bf2f(f2bf(f)); }
__device__ __forceinline__ unsigned pack_bf(float lo, float hi) {
    return (unsigned)f2bf(lo) | ((unsigned)f2bf(hi) << 16);
}
__device__ __forceinline__ u16 f2h(float f) { _Float16 h = (_Float16)f; return __builtin_bit_cast(u16, h); }
typedef __attribute__((ext_vector_type(2))) _Float16 f16x2;
__device__ __forceinline__ unsigned pack_h(float lo, float hi) {
    return (unsigned)f2h(lo) | ((unsigned)f2h(hi) << 16);
}
__device__ __forceinline__ float wave_sum(float v) {
#pragma unroll
    for (int o = 32; o > 0; o >>= 1) v += __shfl_xor(v, o, 64);
    return v;
}
__device__ __forceinline__ float row16_sum(float v) {
    v += __builtin_bit_cast(float, __builtin_amdgcn_update_dpp(0, __builtin_bit_cast(int, v), 0x128, 0xf, 0xf, false));
    v += __builtin_bit_cast(float, __builtin_amdgcn_update_dpp(0, __builtin_bit_cast(int, v), 0x124, 0xf, 0xf, false));
    v += __builtin_bit_cast(float, __builtin_amdgcn_update_dpp(0, __builtin_bit_cast(int, v), 0x122, 0xf, 0xf, false));
    v += __builtin_bit_cast(float, __builtin_amdgcn_update_dpp(0, __builtin_bit_cast(int, v), 0x121, 0xf, 0xf, false));
    return v;
}
__device__ __forceinline__ float dot2bf(unsigned a, unsigned b, float c) {
    return __builtin_amdgcn_fdot2_f32_bf16(__builtin_bit_cast(bf16x2, a), __builtin_bit_cast(bf16x2, b), c, false);
}
__device__ __forceinline__ f32x2 ln_stats(float mean, float var) {
    const float mb = rbf(mean);
    const float sd = rbf(__builtin_sqrtf(rbf(rbf(var) + 1e-5f)));
    return f32x2{mb, 1.0f / sd};
}

#define SIDE_PRIO 2
constexpr int KA_G1 = 256, KA_LN = 128, KA_W2 = 112;
constexpr int KA_GRID = KA_G1 + KA_LN + KA_W2;
constexpr int G1_STAGE = 24 * 1024;
constexpr int G1_AUX = 3 * G1_STAGE;
constexpr int KA_SMEM = G1_AUX + 3072;

__device__ __forceinline__ float gelu_tanh(float x) {
    const float t = x * (-2.302208198f - 0.1029432397f * x * x);
    return x * __builtin_amdgcn_rcpf(1.0f + __builtin_amdgcn_exp2f(t));
}

__device__ __forceinline__ void glds16(unsigned ldsaddr, unsigned voff, const void* sbase) {
    asm volatile("s_mov_b32 m0, %0\n\ts_nop 0\n\tglobal_load_lds_dwordx4 %1, %2"
                 :: "s"(ldsaddr), "v"(voff), "s"(sbase) : "memory");
}
#define DS_RD128(dst, addr, off) asm volatile("ds_read_b128 %0, %1 offset:%2" : "=v"(dst) : "v"(addr), "i"(off))
#define DS_RDTR(dst, addr, off) asm volatile("ds_read_b64_tr_b16 %0, %1 offset:%2" : "=v"(dst) : "v"(addr), "i"(off))
typedef __attribute__((ext_vector_type(16))) float f32x16;

template <int NT>
__device__ __forceinline__ void ka_gemm1_wave(const int w, const int lane, const int mb, const int nb,
                                              const u16* __restrict__ x, const u16* __restrict__ wg,
                                              const u16* __restrict__ wu, u16* __restrict__ hF, char* smem, char* ws) {
    const int lg = w & 3;
    constexpr int fh = NT == 4 ? 0 : 1;
    constexpr int tl0 = NT == 4 ? 0 : 4;
    const unsigned lds0 = (unsigned)(uintptr_t)(LAS char*)smem;
    auto tile_of = [&](int t) { int v = t + lg; v = v >= NT ? v - NT : v; v = v >= NT ? v - NT : v; return tl0 + v; };
    const char* wbase = (const char*)(fh ? wu : wg) + 224 * nb;
    const char* xbase = (const char*)x + (size_t)(128 * mb + 16 * w) * 1024;
    unsigned wv[2], xv;
#pragma unroll
    for (int i = 0; i < 2; ++i) {
        const int r = 8 * lg + 4 * i + (lane >> 4), slot = lane & 15;
        const int sw = (r & 3) | (fh << 2);
        int g = (((slot >> 1) ^ sw) << 1) | (slot & 1);
        g = g > 13 ? 13 : g;
        wv[i] = r * (F_ * 2) + g * 16;
    }
    {
        const int lr = lane >> 2, pos = lane & 3;
        xv = lr * 1024 + ((pos ^ ((lr >> 2) & 3)) * 16);
    }
    const unsigned wdst = lds0 + fh * 8192 + lg * 2048, xdst = lds0 + 16384 + w * 1024;
    auto stage = [&](int ks, unsigned boff) {
        const char* wb = wbase + (size_t)ks * (32 * F_ * 2);
        glds16(wdst + boff, wv[0], wb);
        glds16(wdst + boff + 1024, wv[1], wb);
        glds16(xdst + boff, xv, xbase + ks * 64);
    };
    unsigned aw[NT], ax[2];
    {
        const int gu = (lane >> 4) & 1, h = lane >> 5, r = (lane >> 2) & 3, p = lane & 3;
        const int sw = r | (gu << 2);
#pragma unroll
        for (int t = 0; t < NT; ++t) aw[t] = lds0 + gu * 8192 + (8 * h + r) * 256 + ((tile_of(t) ^ sw) << 5) + 8 * p;
        const int lr = lane & 31;
#pragma unroll
        for (int s2 = 0; s2 < 2; ++s2) ax[s2] = lds0 + 16384 + (32 * lg + lr) * 64 + (((2 * s2 + h) ^ ((lr >> 2) & 3)) * 16);
    }
    f32x16 acc[NT];
#pragma unroll
    for (int t = 0; t < NT; ++t)
#pragma unroll
        for (int i = 0; i < 16; ++i) acc[t][i] = 0.f;
    u32x2 wl[2][NT], wh[2][NT];
    u32x4 xf[2];
    float cs = 0.f, sx = 0.f, sxx = 0.f;
    auto wfrag = [&](int s2, int t) { return u32x4{wl[s2][t][0], wl[s2][t][1], wh[s2][t][0], wh[s2][t][1]}; };
    stage(0, 0);
    stage(1, G1_STAGE);
    stage(2, 2 * G1_STAGE);
    asm volatile("s_waitcnt vmcnt(6)" ::: "memory");
    __builtin_amdgcn_s_barrier();
    SCHED_FENCE();
    STAMP(ws, 0, 1);
    DS_RD128(xf[0], ax[0], 0);
#pragma unroll
    for (int t = 0; t < NT; ++t) { DS_RDTR(wl[0][t], aw[t], 0); DS_RDTR(wh[0][t], aw[t], 1024); }
    DS_RD128(xf[1], ax[1], 0);
#pragma unroll
    for (int t = 0; t < NT; ++t) { DS_RDTR(wl[1][t], aw[t], 4096); DS_RDTR(wh[1][t], aw[t], 5120); }
    SCHED_FENCE();
    float cs2 = 0.f, sx2 = 0.f, sxx2 = 0.f;
    auto colsum = [&](u32x4 v) {
#pragma unroll
        for (int e = 0; e < 4; ++e) { cs += bflo(v[e]); cs2 += bfhi(v[e]); }
        asm volatile("" : "+v"(cs), "+v"(cs2));
    };
    auto rowstat = [&](u32x4 v) {
#pragma unroll
        for (int e = 0; e < 4; ++e) {
            const float lo = bflo(v[e]), hi = bfhi(v[e]);
            sx += lo; sxx = __builtin_fmaf(lo, lo, sxx); sx2 += hi; sxx2 = __builtin_fmaf(hi, hi, sxx2);
        }
        asm volatile("" : "+v"(sx), "+v"(sxx), "+v"(sx2), "+v"(sxx2));
    };
    auto iter = [&](int ks, auto bufc, auto modec) {
        constexpr int BUF = decltype(bufc)::value;
        constexpr int MODE = decltype(modec)::value;
        constexpr int NB = ((BUF + 1) % 3) * G1_STAGE;
        constexpr int PB = ((BUF + 2) % 3) * G1_STAGE;
#pragma unroll
        for (int t = 0; t < NT; ++t) {
            if (t == 0) asm volatile("s_waitcnt lgkmcnt(%5)" : "+v"(wl[0][0]), "+v"(wh[0][0]), "+v"(wl[0][1]), "+v"(wh[0][1]), "+v"(xf[0]) : "i"(4 * NT - 3));
            if (t == 2) {
                if constexpr (NT == 4) asm volatile("s_waitcnt lgkmcnt(%4)" : "+v"(wl[0][2]), "+v"(wh[0][2]), "+v"(wl[0][NT - 1]), "+v"(wh[0][NT - 1]) : "i"(2 * NT + 1));
                else asm volatile("s_waitcnt lgkmcnt(%2)" : "+v"(wl[0][2]), "+v"(wh[0][2]) : "i"(2 * NT + 1));
            }
            acc[t] = __builtin_amdgcn_mfma_f32_32x32x16_bf16(__builtin_bit_cast(bf16x8, wfrag(0, t)), __builtin_bit_cast(bf16x8, xf[0]), acc[t], 0, 0, 0);
            SCHED_FENCE();
            if (t == 0 && MODE == 0 && ks > 0) stage(ks + 2, PB);
            if (t == 1) colsum(wfrag(0, 0));
            if (t == 2 && fh == 0) rowstat(xf[0]);
            SCHED_FENCE();
        }
        if constexpr (NT == 4) asm volatile("s_waitcnt lgkmcnt(0)" : "+v"(xf[1]), "+v"(wl[1][0]), "+v"(wh[1][0]), "+v"(wl[1][1]), "+v"(wh[1][1]),
                                  "+v"(wl[1][2]), "+v"(wh[1][2]), "+v"(wl[1][NT - 1]), "+v"(wh[1][NT - 1]) :: "memory");
        else asm volatile("s_waitcnt lgkmcnt(0)" : "+v"(xf[1]), "+v"(wl[1][0]), "+v"(wh[1][0]), "+v"(wl[1][1]), "+v"(wh[1][1]),
                          "+v"(wl[1][2]), "+v"(wh[1][2]) :: "memory");
        if (MODE < 2) {
            if (MODE == 0) asm volatile("s_waitcnt vmcnt(3)" ::: "memory");
            else asm volatile("s_waitcnt vmcnt(0)" ::: "memory");
            SCHED_FENCE();
            __builtin_amdgcn_s_barrier();
        }
        SCHED_FENCE();
#pragma unroll
        for (int t = 0; t < NT; ++t) {
            acc[t] = __builtin_amdgcn_mfma_f32_32x32x16_bf16(__builtin_bit_cast(bf16x8, wfrag(1, t)), __builtin_bit_cast(bf16x8, xf[1]), acc[t], 0, 0, 0);
            SCHED_FENCE();
            if (MODE < 2) {
                if (t == 0) {
                    DS_RD128(xf[0], ax[0], NB);
#pragma unroll
                    for (int u = 0; u < 2; ++u) { DS_RDTR(wl[0][u], aw[u], NB); DS_RDTR(wh[0][u], aw[u], NB + 1024); }
                } else if (t == 1) {
#pragma unroll
                    for (int u = 2; u < NT; ++u) { DS_RDTR(wl[0][u], aw[u], NB); DS_RDTR(wh[0][u], aw[u], NB + 1024); }
                }
            }
            if (t == NT - 2) colsum(wfrag(1, 0));
            if (t == NT - 1 && fh == 1) rowstat(xf[1]);
            SCHED_FENCE();
        }
        if (MODE < 2) {
            DS_RD128(xf[1], ax[1], NB);
#pragma unroll
            for (int t = 0; t < NT; ++t) { DS_RDTR(wl[1][t], aw[t], NB + 4096); DS_RDTR(wh[1][t], aw[t], NB + 5120); }
        }
        SCHED_FENCE();
    };
    using I0 = std::integral_constant<int, 0>; using I1 = std::integral_constant<int, 1>; using I2 = std::integral_constant<int, 2>;
#pragma unroll 1
    for (int k3 = 0; k3 < 12; k3 += 3) {
        iter(k3, I0{}, I0{});
        iter(k3 + 1, I1{}, I0{});
        iter(k3 + 2, I2{}, I0{});
    }
    iter(12, I0{}, I0{});
    iter(13, I1{}, I0{});
    iter(14, I2{}, I1{});
    iter(15, I0{}, I2{});
    STAMP(ws, 0, 2);
    f32x2* stp = (f32x2*)(smem + G1_AUX);
    float* cst = (float*)(smem + G1_AUX + 2048);
    sx += sx2; sxx += sxx2; cs += cs2;
    sx += __shfl_xor(sx, 32, 64); sxx += __shfl_xor(sxx, 32, 64); cs += __shfl_xor(cs, 32, 64);
    if (lane < 32) {
        stp[fh * 128 + 32 * lg + lane] = f32x2{sx, sxx};
        cst[tile_of(0) * 32 + lane] = cs;
    }
    __syncthreads();
    f32x2 st;
    {
        const f32x2 p0 = stp[32 * lg + (lane & 31)], p1 = stp[128 + 32 * lg + (lane & 31)];
        float mean = (p0[0] + p1[0]) * (1.0f / D_);
        float var = fmaxf((p0[1] + p1[1]) * (1.0f / D_) - mean * mean, 0.f);
        if (__any(mean * mean > 32.0f * var)) {
            for (int r = 0; r < 32; ++r) {
                const u32x4 raw = *(const u32x4*)(x + (size_t)(128 * mb + 32 * lg + r) * D_ + lane * 8);
                float v[8], s = 0.f, q2 = 0.f;
#pragma unroll
                for (int e = 0; e < 4; ++e) { v[2 * e] = bflo(raw[e]); v[2 * e + 1] = bfhi(raw[e]); }
#pragma unroll
                for (int e = 0; e < 8; ++e) s += v[e];
                s = wave_sum(s) * (1.0f / D_);
#pragma unroll
                for (int e = 0; e < 8; ++e) q2 += (v[e] - s) * (v[e] - s);
                q2 = wave_sum(q2) * (1.0f / D_);
                if ((lane & 31) == r) { mean = s; var = q2; }
            }
        }
        st = ln_stats(mean, var);
    }
    const int hh = lane >> 5;
    const int lt = 8 * mb + 2 * lg + ((lane >> 4) & 1);
#pragma unroll
    for (int t = 0; t < NT; ++t) {
        const int T = 7 * nb + tile_of(t);
        const float* cp = cst + tile_of(t) * 32 + 4 * hh;
        u32x4 p;
#pragma unroll
        for (int i2 = 0; i2 < 2; ++i2) {
            const f32x4 cg = *(const f32x4*)(cp + 8 * i2), cu = *(const f32x4*)(cp + 16 + 8 * i2);
            float hv[4];
#pragma unroll
            for (int r = 0; r < 4; ++r) {
                const float g = rbf((acc[t][4 * i2 + r] - st[0] * cg[r]) * st[1]);
                const float u = rbf((acc[t][8 + 4 * i2 + r] - st[0] * cu[r]) * st[1]);
                hv[r] = rbf(gelu_tanh(g)) * u;
            }
            p[2 * i2] = pack_bf(hv[0], hv[1]);
            p[2 * i2 + 1] = pack_bf(hv[2], hv[3]);
        }
        gstore((char*)hF + ((size_t)(lt * 56 + (T >> 1)) * 64 + (2 * (T & 1) + hh) * 16 + (lane & 15)) * 16, p);
    }
}

__device__ __forceinline__ void ka_ln(int b, const u16* __restrict__ x, u16* __restrict__ xnT, f32x2* __restrict__ stat,
                                      char* smem) {
    const int tid = threadIdx.x, w = tid >> 6, lane = tid & 63, rr = lane >> 5, c = lane & 31;
    constexpr int TS = 516;
    u16* tile = (u16*)smem;
    const int l0 = b * 16, rl = 2 * w + rr, row = l0 + rl;
    u32x4 raw[2];
#pragma unroll
    for (int i = 0; i < 2; ++i) raw[i] = *(const u32x4*)(x + (size_t)row * D_ + (c + 32 * i) * 8);
    float v[16];
#pragma unroll
    for (int i = 0; i < 2; ++i)
#pragma unroll
        for (int j = 0; j < 4; ++j) { v[8 * i + 2 * j] = bflo(raw[i][j]); v[8 * i + 2 * j + 1] = bfhi(raw[i][j]); }
    float s = 0.f;
#pragma unroll
    for (int j = 0; j < 16; ++j) s += v[j];
    s = row16_sum(s); s += __shfl_xor(s, 16, 64);
    const float mean = s * (1.0f / D_);
    float q2 = 0.f;
#pragma unroll
    for (int j = 0; j < 16; ++j) { float dlt = v[j] - mean; q2 += dlt * dlt; }
    q2 = row16_sum(q2); q2 += __shfl_xor(q2, 16, 64);
    const f32x2 st = ln_stats(mean, q2 * (1.0f / D_));
    if (c == 0) gstore(stat + row, st);
#pragma unroll
    for (int i = 0; i < 2; ++i) {
        u32x4 ph;
#pragma unroll
        for (int j = 0; j < 4; ++j)
            ph[j] = pack_h(rbf(rbf(v[8 * i + 2 * j] - st[0]) * st[1]), rbf(rbf(v[8 * i + 2 * j + 1] - st[0]) * st[1]));
        u16* tp = tile + rl * TS + (c + 32 * i) * 8;
        *(u32x2*)(tp) = u32x2{ph[0], ph[1]};
        *(u32x2*)(tp + 4) = u32x2{ph[2], ph[3]};
    }
    __syncthreads();
#pragma unroll
    for (int it = 0; it < 2; ++it) {
        int d = it * 256 + (tid >> 1), part = tid & 1;
        const u16* sp = tile + (8 * part) * TS + d;
        u32x4 o;
#pragma unroll
        for (int j = 0; j < 4; ++j) o[j] = (unsigned)sp[(2 * j) * TS] | ((unsigned)sp[(2 * j + 1) * TS] << 16);
        gstore(xnT + (size_t)d * L_ + l0 + 8 * part, o);
    }
}

__device__ __forceinline__ void ka_w2(int b, const u16* __restrict__ wd, u16* __restrict__ W2, char* smem) {
    const int tid = threadIdx.x, ks = b >> 1, dh = b & 1;
    u16* tile = (u16*)smem;
    u32x4 v[2];
#pragma unroll
    for (int i = 0; i < 2; ++i) {
        int c = tid + 512 * i, r = c >> 5, ch = c & 31;
        v[i] = *(const u32x4*)(wd + (size_t)(32 * ks + r) * D_ + 256 * dh + ch * 8);
    }
    SCHED_FENCE();
#pragma unroll
    for (int i = 0; i < 2; ++i) {
        int c = tid + 512 * i, r = c >> 5, ch = c & 31;
        *(u32x4*)(tile + r * 264 + ch * 8) = v[i];
    }
    __syncthreads();
#pragma unroll
    for (int i = 0; i < 2; ++i) {
        int dbl = 2 * i + (tid >> 8), t = (tid >> 6) & 3, lane = tid & 63, r = lane & 15, q = lane >> 4;
        int dl = 64 * dbl + 16 * (r >> 2) + 4 * t + (r & 3);
        const u16* sp = tile + (16 * (q >> 1) + 4 * (q & 1)) * 264 + dl;
        u32x4 o4;
        o4[0] = (unsigned)sp[0 * 264] | ((unsigned)sp[1 * 264] << 16);
        o4[1] = (unsigned)sp[2 * 264] | ((unsigned)sp[3 * 264] << 16);
        o4[2] = (unsigned)sp[8 * 264] | ((unsigned)sp[9 * 264] << 16);
        o4[3] = (unsigned)sp[10 * 264] | ((unsigned)sp[11 * 264] << 16);
        gstore(W2 + ((size_t)((4 * dh + dbl) * 56 + ks) * 4 + t) * 512 + lane * 8, o4);
    }
}

__global__ __launch_bounds__(512, 4) void ka_main(const u16* __restrict__ x, const u16* __restrict__ wg,
                                                  const u16* __restrict__ wu, const u16* __restrict__ wd,
                                                  char* __restrict__ ws) {
    __shared__ __attribute__((aligned(16))) char smem[KA_SMEM];
    int b = blockIdx.x;
    STAMP(ws, 0, 0);
    if (b < KA_G1) {
        const int lane = threadIdx.x & 63;
        const int w = __builtin_amdgcn_readfirstlane(threadIdx.x >> 6);
        const int xcd = b & 7, i5 = b >> 3;
        const int mb = (xcd & 1) * 8 + (i5 & 7), nb = (xcd >> 1) * 4 + (i5 >> 3);
        if (w < 4) ka_gemm1_wave<4>(w, lane, mb, nb, x, wg, wu, (u16*)(ws + WS_HF), smem, ws);
        else ka_gemm1_wave<3>(w, lane, mb, nb, x, wg, wu, (u16*)(ws + WS_HF), smem, ws);
    } else if (b < KA_G1 + KA_LN) {
        __builtin_amdgcn_s_setprio(SIDE_PRIO);
        ka_ln(b - KA_G1, x, (u16*)(ws + WS_XNT), (f32x2*)(ws + WS_STAT), smem);
    } else {
        b -= KA_G1 + KA_LN;
        __builtin_amdgcn_s_setprio(SIDE_PRIO);
        if (b == 0) {
#pragma unroll
            for (int i = 0; i < 8; ++i) ((unsigned*)(ws + WS_FLAG))[threadIdx.x + 512 * i] = 0u;
        }
        ka_w2(b, wd, (u16*)(ws + WS_W2), smem);
    }
    STAMP(ws, 0, 3);
}

constexpr int KB_CV = 512, KB_G2 = 256;
constexpr int CV_KD = 42784;
constexpr int CV_COPY = 4160;
constexpr int CV_XOFF = 8 * CV_COPY;
constexpr int KB_SMEM = 49152;

struct IdxTab { u16 v[2064]; };
constexpr IdxTab make_idx_tab() {
    IdxTab t{};
    for (int i = 0; i < 2064; ++i) {
        int lag = 2047 - i;
        if (lag < 0) { t.v[i] = 1024; continue; }
        int p = lag;
        if (lag >= 256) {
            int s = 0;
            while ((lag >> s) >= 256) ++s;
            int step = 1 << s, rem = lag & (step - 1), base = lag - rem, half = step >> 1;
            if (rem > half || (rem == half && ((base >> s) & 1))) base += step;
            p = base;
        }
        int e = 0, r = p;
        if (p >= 256) {
            while ((p >> e) >= 256) ++e;
            if (e > 3) e = 3;
            r = (p - (128 << e)) >> e;
        }
        t.v[i] = (u16)(e * 256 + (r & 15) * 16 + (r >> 4));
    }
    return t;
}
__device__ const IdxTab kIdxTab = make_idx_tab();

__device__ __forceinline__ int bset_idx(int e) {
    if (e < 16) return e;
    int s = (31 - __builtin_clz(e)) - 3;
    return 8 + 8 * s + ((e - (8 << s)) >> s);
}
struct cplx { float r, i; };
__device__ __forceinline__ cplx cmul(cplx a, cplx b) { return cplx{a.r * b.r - a.i * b.i, a.r * b.i + a.i * b.r}; }
__device__ __forceinline__ cplx cexp_p(float zr2, float zi2, float p) {
    float mag = __builtin_amdgcn_exp2f(zr2 * p);
    float ph = __builtin_amdgcn_fractf(zi2 * p);
    return cplx{mag * __builtin_amdgcn_cosf(ph), mag * __builtin_amdgcn_sinf(ph)};
}

constexpr int CV_RS = 528;
__device__ __forceinline__ float kb_kgen(const int d, const u16* __restrict__ lar, const u16* __restrict__ aim,
                                         const u16* __restrict__ cre, const u16* __restrict__ cim,
                                         const u16* __restrict__ ldt, char* __restrict__ ws, char* smem, u16* Kd) {
    const int tid = threadIdx.x, lane = tid & 63;
    const int w = __builtin_amdgcn_readfirstlane(tid >> 6);
    char* Ut = smem;
    char* Vt = smem + 41 * CV_RS;
    const int n = (w & 1) * 64 + lane;
    const u16 r_ldt = ldt[d];
    const u16 r_c0 = cre[d * NS_ + lane], r_c1 = cim[d * NS_ + lane], r_c2 = cre[d * NS_ + 64 + lane], r_c3 = cim[d * NS_ + 64 + lane];
    const u16 r_lar = lar[d * NS_ + n], r_aim = aim[d * NS_ + n];
    SCHED_FENCE();
    const float dt = rbf(__expf(bf2f(r_ldt)));
    float sabs;
    {
        const float rs = row16_sum(fabsf(bf2f(r_c0)) + fabsf(bf2f(r_c1)) + fabsf(bf2f(r_c2)) + fabsf(bf2f(r_c3)));
        const int ri = __builtin_bit_cast(int, rs);
        sabs = (__builtin_bit_cast(float, __builtin_amdgcn_readlane(ri, 0)) + __builtin_bit_cast(float, __builtin_amdgcn_readlane(ri, 16)) +
                __builtin_bit_cast(float, __builtin_amdgcn_readlane(ri, 32)) + __builtin_bit_cast(float, __builtin_amdgcn_readlane(ri, 48))) * dt;
    }
    int ex = (int)((__float_as_uint(sabs) >> 23) & 0xff);
    int se = 127 + 14 - (ex - 126);
    se = se < 1 ? 1 : (se > 254 ? 254 : se);
    const float sc = __uint_as_float((unsigned)se << 23);
    STAMP(ws, 1, 5);
    {
        const float ar = -rbf(__expf(bf2f(r_lar)));
        const float zr = ar * dt, zi = bf2f(r_aim) * dt;
        const bool isU = w >= 2;
        const float pb = isU ? 16.0f : 1.0f;
        const float zr2 = zr * 1.4426950408889634f;
        const float zi2 = zi * (isU ? -0.15915494309189535f : 0.15915494309189535f);
        cplx e[8];
#pragma unroll
        for (int k = 0; k < 8; ++k) e[k] = cexp_p(zr2, zi2, pb * (float)(1 << k));
        cplx v[41];
        {
            const float crv = bf2f((w & 1) ? r_c2 : r_c0), civ = bf2f((w & 1) ? r_c3 : r_c1);
            v[0] = isU ? cplx{crv * dt * sc, -(civ * dt * sc)} : cplx{1.0f, 0.0f};
        }
        char* tb = (isU ? Ut : Vt) + n * 4;
        auto put = [&](int i0, int i1) {
#pragma unroll
            for (int i = i0; i < i1; ++i) *(unsigned*)(tb + i * CV_RS) = pack_h(v[i].r, v[i].i);
            SCHED_FENCE();
        };
        v[1] = cmul(v[0], e[0]);
#pragma unroll
        for (int k = 0; k < 2; ++k) v[2 + k] = cmul(v[k], e[1]);
#pragma unroll
        for (int k = 0; k < 4; ++k) v[4 + k] = cmul(v[k], e[2]);
#pragma unroll
        for (int k = 0; k < 8; ++k) v[8 + k] = cmul(v[k], e[3]);
        v[40] = cmul(v[0], e[7]);
        if (isU) *(unsigned*)(tb + 40 * CV_RS) = pack_h(v[40].r, v[40].i);
        put(0, 16);
#pragma unroll
        for (int k = 0; k < 8; ++k) v[16 + k] = cmul(v[2 * k], e[4]);
        put(16, 24);
#pragma unroll
        for (int k = 0; k < 8; ++k) v[24 + k] = cmul(v[k < 4 ? 4 * k : 16 + (4 * k - 16) / 2], e[5]);
        put(24, 32);
#pragma unroll
        for (int k = 0; k < 8; ++k) {
            const int ee = 8 * k;
            const int src = ee < 16 ? ee : (ee < 32 ? 16 + (ee - 16) / 2 : 24 + (ee - 32) / 4);
            v[32 + k] = cmul(v[src], e[6]);
        }
        put(32, 40);
    }
    STAMP(ws, 1, 7);
    __syncthreads();
    const int j = lane & 15, q = lane >> 4;
    {
        const int urow = (w == 0) ? j : (8 + 8 * w + j);
        const int vrow = bset_idx(j << w);
        const char* ua = Ut + urow * CV_RS + q * 16;
        const char* vb = Vt + vrow * CV_RS + q * 16;
        f16x8 a[8], bb[8];
#pragma unroll
        for (int ks = 0; ks < 8; ++ks) { a[ks] = *(const f16x8*)(ua + ks * 64); bb[ks] = *(const f16x8*)(vb + ks * 64); }
        SCHED_FENCE();
        f32x4 acc0 = {0.f, 0.f, 0.f, 0.f}, acc1 = {0.f, 0.f, 0.f, 0.f};
#pragma unroll
        for (int ks = 0; ks < 8; ks += 2) {
            acc0 = __builtin_amdgcn_mfma_f32_16x16x32_f16(a[ks], bb[ks], acc0, 0, 0, 0);
            acc1 = __builtin_amdgcn_mfma_f32_16x16x32_f16(a[ks + 1], bb[ks + 1], acc1, 0, 0, 0);
        }
        u32x2 o = {pack_h(acc0[0] + acc1[0], acc0[1] + acc1[1]), pack_h(acc0[2] + acc1[2], acc0[3] + acc1[3])};
        *(u32x2*)(Kd + w * 256 + j * 16 + 4 * q) = o;
        if (tid == 0) Kd[1024] = 0;
    }
    return 2.0f / sc;
}

__device__ __forceinline__ void kb_conv(const int d, const u16* __restrict__ lar, const u16* __restrict__ aim,
                                        const u16* __restrict__ cre, const u16* __restrict__ cim,
                                        const u16* __restrict__ ldt, char* __restrict__ ws, char* smem) {
    const int tid = threadIdx.x, lane = tid & 63;
    const int w = __builtin_amdgcn_readfirstlane(tid >> 6);
    __builtin_amdgcn_s_setprio(2);
    u16* Kd = (u16*)(smem + CV_KD);
    const u32x4 it0 = *(const u32x4*)(kIdxTab.v + 8 * tid), it1 = *(const u32x4*)(kIdxTab.v + 8 * tid + 8);
    const u32x4 xrow = *(const u32x4*)((const u16*)(ws + WS_XNT) + (size_t)d * L_ + tid * 8);
    const float osc = kb_kgen(d, lar, aim, cre, cim, ldt, ws, smem, Kd);
    __syncthreads();
    STAMP(ws, 1, 1);
    const int j = lane & 15, q = lane >> 4;
    {
        unsigned wd[8];
#pragma unroll
        for (int e = 0; e < 4; ++e) {
            wd[e] = (unsigned)Kd[it0[e] & 0xffff] | ((unsigned)Kd[it0[e] >> 16] << 16);
            wd[4 + e] = (unsigned)Kd[it1[e] & 0xffff] | ((unsigned)Kd[it1[e] >> 16] << 16);
        }
        char* dst = smem + tid * 16;
#pragma unroll
        for (int e = 0; e < 4; ++e) {
            u32x4 ev = {wd[e], wd[e + 1], wd[e + 2], wd[e + 3]};
            *(u32x4*)(dst + (2 * e) * CV_COPY) = ev;
            u32x4 od;
#pragma unroll
            for (int k = 0; k < 4; ++k) od[k] = __builtin_amdgcn_alignbit(wd[e + k + 1], wd[e + k], 16);
            *(u32x4*)(dst + (2 * e + 1) * CV_COPY) = od;
        }
        char* xp = smem + CV_XOFF;
        if (tid < 32) *(u32x4*)(xp + tid * 16) = u32x4{0u, 0u, 0u, 0u};
        if (tid >= 64 && tid < 72) *(u32x4*)(smem + (tid - 64) * CV_COPY + 256 * 16) = u32x4{0u, 0u, 0u, 0u};
        *(u32x4*)(xp + 512 + tid * 16) = xrow;
    }
    __syncthreads();
    STAMP(ws, 1, 2);
    const char* kb = smem + ((7 - j) & 7) * CV_COPY + (253 + q - (j >> 3)) * 16 - w * 2 * 64;
    const char* xq = smem + CV_XOFF + (256 + 16 * j - 16 + 8 * q) * 2 - w * 2 * 64;
    f32x4 acc[8];
#pragma unroll
    for (int i = 0; i < 8; ++i) acc[i] = f32x4{0.f, 0.f, 0.f, 0.f};
    {
        f16x8 bfr[2][8], af[3];
#pragma unroll
        for (int c = 0; c < 8; ++c) bfr[0][c] = *(const f16x8*)(xq + c * 512);
        af[0] = *(const f16x8*)(kb);
        af[1] = *(const f16x8*)(kb - 8 * 64);
        SCHED_FENCE();
#pragma unroll
        for (int ri = 0; ri < 2; ++ri) {
#pragma unroll
            for (int mm = 0; mm < 8; ++mm) {
                const int g = ri * 8 + mm;
                if (g + 2 < 16) {
                    const int g2 = g + 2;
                    af[g2 % 3] = *(const f16x8*)(kb - (8 * (g2 & 7) + (g2 >> 3)) * 64);
                }
                if (ri < 1) bfr[1][mm] = *(const f16x8*)(xq + mm * 512 - 64);
#pragma unroll
                for (int ct = mm; ct < 8; ++ct)
                    acc[ct] = __builtin_amdgcn_mfma_f32_16x16x32_f16(af[g % 3], bfr[ri][ct - mm], acc[ct], 0, 0, 0);
                SCHED_FENCE();
            }
        }
    }
    STAMP(ws, 1, 4);
    __syncthreads();
    {
        f32x4* exb = (f32x4*)(smem + w * 8192) + lane;
#pragma unroll
        for (int i = 0; i < 8; ++i) exb[i * 64] = acc[i];
    }
    __syncthreads();
    {
        u16* yp = (u16*)(ws + WS_YT) + (size_t)d * L_ + 16 * j + 4 * q + 512 * w;
#pragma unroll
        for (int i = 0; i < 2; ++i) {
            const f32x4* exb = (const f32x4*)(smem + (2 * w + i) * 1024) + lane;
            f32x4 o = exb[0] + exb[512] + exb[1024] + exb[1536];
            u32x2 p = {pack_bf(o[0] * osc, o[1] * osc), pack_bf(o[2] * osc, o[3] * osc)};
            gstore(yp + 256 * i, p);
        }
    }
    STAMP(ws, 1, 6);
    asm volatile("s_waitcnt vmcnt(0)" ::: "memory");
    __syncthreads();
    if (tid < 8) __hip_atomic_fetch_add((GAS unsigned*)(ws + WS_FLAG) + 64 * (8 * (d >> 6) + tid), 1u, __ATOMIC_RELAXED, __HIP_MEMORY_SCOPE_AGENT);
}

__device__ __forceinline__ void kb_gemm2(const int bid, const u16* __restrict__ x, const u16* __restrict__ dskip,
                                         char* __restrict__ ws, u16* __restrict__ out, char* smem) {
    const int tid = threadIdx.x, lane = tid & 63;
    const int w = __builtin_amdgcn_readfirstlane(tid >> 6);
    const int xcd = bid & 7, i5 = bid >> 3;
    const int lb = 4 * xcd + (i5 & 3), db = i5 >> 2;
    const int r = lane & 15, q = lane >> 4;
    const int lrow = 64 * lb + 16 * w + r;
    const int dcol = 64 * db + 16 * q;
    f32x4 acc[4][4];
#pragma unroll
    for (int t = 0; t < 4; ++t)
#pragma unroll
        for (int i = 0; i < 4; ++i) acc[t][i] = f32x4{0.f, 0.f, 0.f, 0.f};
    const __amdgpu_buffer_rsrc_t wrs = __builtin_amdgcn_make_buffer_rsrc(ws + WS_W2, 0, F_ * D_ * 2, 0x00020000);
    const __amdgpu_buffer_rsrc_t hrs = __builtin_amdgcn_make_buffer_rsrc(ws + WS_HF, 0, L_ * F_ * 2, 0x00020000);
    const int wso = (db * 56 + 14 * w) * 4096, hso = (4 * lb * 56 + 14 * w) * 1024;
    const int l16 = lane * 16;
    auto ldw = [&](int k, int t) { return __builtin_bit_cast(bf16x8, __builtin_amdgcn_raw_buffer_load_b128(wrs, l16, wso + (k * 4 + t) * 1024, 0)); };
    auto ldh = [&](int k, int i) { return __builtin_bit_cast(bf16x8, __builtin_amdgcn_raw_buffer_load_b128(hrs, l16, hso + (i * 56 + k) * 1024, 0)); };
#define G2_PD 2
    constexpr int PD = G2_PD;
    bf16x8 wf[PD][4], hf[PD][4];
#pragma unroll
    for (int s = 0; s < PD; ++s) {
#pragma unroll
        for (int t = 0; t < 4; ++t) wf[s][t] = ldw(s, t);
#pragma unroll
        for (int i = 0; i < 4; ++i) hf[s][i] = ldh(s, i);
    }
    SCHED_FENCE();
    const __amdgpu_buffer_rsrc_t yrs = __builtin_amdgcn_make_buffer_rsrc(ws + WS_YT, 0, D_ * L_ * 2, 0x00020000);
    auto poll = [&]() {
        return __hip_atomic_load((GAS unsigned*)(ws + WS_FLAG) + 64 * (8 * db + xcd), __ATOMIC_RELAXED, __HIP_MEMORY_SCOPE_AGENT);
    };
    auto ldy = [&](u32x4 (&yv)[2]) {
        int ln = l16; asm volatile("" : "+v"(ln));
        const int yo = (ln >> 5) * (L_ * 2) + (ln & 16);
        const int so = (64 * db * L_ + 64 * lb + 16 * w) * 2;
        yv[0] = __builtin_amdgcn_raw_buffer_load_b128(yrs, yo, so, 16);
        yv[1] = __builtin_amdgcn_raw_buffer_load_b128(yrs, yo, so + 32 * L_ * 2, 16);
    };
    unsigned fv = 0;
    bool early = false;
    u32x4 yv[2];
    u32x4 xv[2], dv[2];
    f32x2 st;
#pragma unroll
    for (int k = 0; k < 14; ++k) {
        const int s = k % PD;
#pragma unroll
        for (int t = 0; t < 4; ++t) {
#pragma unroll
            for (int i = 0; i < 4; ++i)
                acc[t][i] = __builtin_amdgcn_mfma_f32_16x16x32_bf16(wf[s][t], hf[s][i], acc[t][i], 0, 0, 0);
            SCHED_FENCE();
            if (k + PD < 14) wf[s][t] = ldw(k + PD, t);
            SCHED_FENCE();
        }
        if (k + PD < 14) {
#pragma unroll
            for (int i = 0; i < 4; ++i) hf[s][i] = ldh(k + PD, i);
        }
        if (k == 10) fv = poll();
        if (k == 12) {
            early = __builtin_amdgcn_readfirstlane(fv) == 64u;
            if (early) ldy(yv);
        }
        if (k == 13) {
#pragma unroll
            for (int i = 0; i < 2; ++i) {
                xv[i] = *(const u32x4*)(x + (size_t)lrow * D_ + dcol + 8 * i);
                dv[i] = *(const u32x4*)(dskip + dcol + 8 * i);
            }
            st = ((const f32x2*)(ws + WS_STAT))[lrow];
        }
        SCHED_FENCE();
        if (k == 0) STAMP(ws, 1, 1);
    }
    STAMP(ws, 1, 2);
    {
        f32x4* red = (f32x4*)smem;
#pragma unroll
        for (int i = 0; i < 4; ++i) {
            if (i != w) {
                const int si = w < i ? w : w - 1;
#pragma unroll
                for (int t = 0; t < 4; ++t) red[((i * 3 + si) * 4 + t) * 64 + lane] = acc[t][i];
            }
        }
    }
    __syncthreads();
    STAMP(ws, 1, 4);
    f32x4 f[4];
    {
        const f32x4* red = (const f32x4*)smem;
#pragma unroll
        for (int t = 0; t < 4; ++t) f[t] = w == 0 ? acc[t][0] : (w == 1 ? acc[t][1] : (w == 2 ? acc[t][2] : acc[t][3]));
#pragma unroll
        for (int si = 0; si < 3; ++si)
#pragma unroll
            for (int t = 0; t < 4; ++t) f[t] += red[((w * 3 + si) * 4 + t) * 64 + lane];
    }
    float gt[16], sk[16];
#pragma unroll
    for (int e = 0; e < 16; ++e) {
        const float fv2 = rbf(f[e >> 2][e & 3]);
        gt[e] = rbf(fv2 * __builtin_amdgcn_rcpf(1.0f + __expf(-fv2)));
        const unsigned dw = dv[e >> 3][(e >> 1) & 3], xw = xv[e >> 3][(e >> 1) & 3];
        const float ds = (e & 1) ? bfhi(dw) : bflo(dw);
        const float xx = (e & 1) ? bfhi(xw) : bflo(xw);
        sk[e] = rbf(rbf(rbf(xx - st[0]) * st[1]) * ds);
        asm volatile("" : "+v"(gt[e]), "+v"(sk[e]));
    }
    if (w == 0 && !early) {
        for (unsigned spins = 0; spins < (1u << 22); ++spins) {
            if (__builtin_amdgcn_readfirstlane(poll()) == 64u) break;
            __builtin_amdgcn_s_sleep(4);
        }
    }
    __syncthreads();
    STAMP(ws, 1, 5);
    if (!early) ldy(yv);
    u16 yt[16];
    {
        char* yb = smem + w * 12288;
        *(u32x4*)(yb + lane * 16) = yv[0];
        *(u32x4*)(yb + 1024 + lane * 16) = yv[1];
        asm volatile("s_waitcnt lgkmcnt(0)" ::: "memory");
        const u16* yr = (const u16*)(yb + q * 512 + 2 * r);
#pragma unroll
        for (int e = 0; e < 16; ++e) yt[e] = yr[e * 16];
    }
#pragma unroll
    for (int i = 0; i < 2; ++i) {
        u32x4 ov;
#pragma unroll
        for (int jj = 0; jj < 4; ++jj) {
            float res[2];
#pragma unroll
            for (int hh = 0; hh < 2; ++hh) {
                const int e = 8 * i + 2 * jj + hh;
                const float xx = hh ? bfhi(xv[i][jj]) : bflo(xv[i][jj]);
                res[hh] = xx + rbf(rbf(bf2f(yt[e]) + sk[e]) * gt[e]);
            }
            ov[jj] = pack_bf(res[0], res[1]);
        }
        *(u32x4*)(out + (size_t)lrow * D_ + dcol + 8 * i) = ov;
    }
}

__global__ __launch_bounds__(256, 3) void kb_main(const u16* __restrict__ x, const u16* __restrict__ lar,
                                                  const u16* __restrict__ aim, const u16* __restrict__ cre,
                                                  const u16* __restrict__ cim, const u16* __restrict__ ldt,
                                                  const u16* __restrict__ dskip, char* __restrict__ ws,
                                                  u16* __restrict__ out) {
    __shared__ __attribute__((aligned(16))) char smem[KB_SMEM];
    const int b = blockIdx.x;
    STAMP(ws, 1, 0);
    const int j = b >> 3, j3 = j / 3, jr = j - 3 * j3;
    if (jr < 2) kb_conv((2 * j3 + jr) * 8 + (b & 7), lar, aim, cre, cim, ldt, ws, smem);
    else kb_gemm2(j3 * 8 + (b & 7), x, dskip, ws, out, smem);
    STAMP(ws, 1, 3);
}

extern "C" void kernel_launch(void* const* d_in, const int* in_sizes, int n_in,
                              void* d_out, int out_size, void* d_ws, size_t ws_size,
                              hipStream_t stream) {
    const u16* x   = (const u16*)d_in[0];
    const u16* lar = (const u16*)d_in[1];
    const u16* aim = (const u16*)d_in[2];
    const u16* cre = (const u16*)d_in[3];
    const u16* cim = (const u16*)d_in[4];
    const u16* ldt = (const u16*)d_in[5];
    const u16* dsk = (const u16*)d_in[6];
    const u16* wg  = (const u16*)d_in[8];
    const u16* wu  = (const u16*)d_in[9];
    const u16* wd  = (const u16*)d_in[10];
    char* ws = (char*)d_ws;
    ka_main<<<KA_GRID, 512, 0, stream>>>(x, wg, wu, wd, ws);
    kb_main<<<KB_CV + KB_G2, 256, 0, stream>>>(x, lar, aim, cre, cim, ldt, dsk, ws, (u16*)d_out);
}
```

```cpp
#include <hip/hip_runtime.h>
#include <hip/hip_bf16.h>
#include <stdint.h>
#include <type_traits>

typedef __attribute__((ext_vector_type(8))) __bf16 bf16x8;
typedef __attribute__((ext_vector_type(2))) __bf16 bf16x2;
typedef __attribute__((ext_vector_type(8))) _Float16 f16x8;
typedef __attribute__((ext_vector_type(4))) float f32x4;
typedef __attribute__((ext_vector_type(2))) float f32x2;
typedef __attribute__((ext_vector_type(4))) unsigned int u32x4;
typedef __attribute__((ext_vector_type(2))) unsigned int u32x2;
typedef unsigned short u16;
#define GAS __attribute__((address_space(1)))
#define LAS __attribute__((address_space(3)))

constexpr int L_ = 2048, D_ = 512, NS_ = 128, F_ = 1792;

constexpr size_t WS_XNT  = 0;
constexpr size_t WS_STAT = 2u << 20;
constexpr size_t WS_FLAG = (2u << 20) + 65536;
constexpr size_t WS_W2   = 3u << 20;
constexpr size_t WS_HF   = 5u << 20;
constexpr size_t WS_YT   = 13u << 20;
constexpr size_t WS_KD   = 15u << 20;
constexpr size_t WS_KSC  = 17u << 20;
constexpr size_t WS_XNF  = 18u << 20;
constexpr size_t WS_W1   = 20u << 20;

#define STAMP(ws_, kid, slot) do {} while (0)
#define SCHED_FENCE() __builtin_amdgcn_sched_barrier(0)

template <typename T> __device__ __forceinline__ void gstore(void* p, T v) {
    if constexpr (sizeof(T) == 16) asm volatile("global_store_dwordx4 %0, %1, off sc0 sc1\n\ts_nop 1" :: "v"(p), "v"(v));
    else asm volatile("global_store_dwordx2 %0, %1, off sc0 sc1\n\ts_nop 1" :: "v"(p), "v"(v));
}
__device__ __forceinline__ float bf2f(u16 v) { return __uint_as_float(((unsigned)v) << 16); }
__device__ __forceinline__ float bflo(unsigned v) { return __uint_as_float(v << 16); }
__device__ __forceinline__ float bfhi(unsigned v) { return __uint_as_float(v & 0xffff0000u); }
__device__ __forceinline__ u16 f2bf(float f) { __bf16 b = (__bf16)f; return __builtin_bit_cast(u16, b); }
__device__ __forceinline__ float rbf(float f) { return bf2f(f2bf(f)); }
__device__ __forceinline__ unsigned pack_bf(float lo, float hi) {
    return (unsigned)f2bf(lo) | ((unsigned)f2bf(hi) << 16);
}
__device__ __forceinline__ u16 f2h(float f) { _Float16 h = (_Float16)f; return __builtin_bit_cast(u16, h); }
typedef __attribute__((ext_vector_type(2))) _Float16 f16x2;
__device__ __forceinline__ unsigned pack_h(float lo, float hi) {
    return (unsigned)f2h(lo) | ((unsigned)f2h(hi) << 16);
}
__device__ __forceinline__ float wave_sum(float v) {
#pragma unroll
    for (int o = 32; o > 0; o >>= 1) v += __shfl_xor(v, o, 64);
    return v;
}
__device__ __forceinline__ float row16_sum(float v) {
    v += __builtin_bit_cast(float, __builtin_amdgcn_update_dpp(0, __builtin_bit_cast(int, v), 0x128, 0xf, 0xf, false));
    v += __builtin_bit_cast(float, __builtin_amdgcn_update_dpp(0, __builtin_bit_cast(int, v), 0x124, 0xf, 0xf, false));
    v += __builtin_bit_cast(float, __builtin_amdgcn_update_dpp(0, __builtin_bit_cast(int, v), 0x122, 0xf, 0xf, false));
    v += __builtin_bit_cast(float, __builtin_amdgcn_update_dpp(0, __builtin_bit_cast(int, v), 0x121, 0xf, 0xf, false));
    return v;
}
__device__ __forceinline__ float dot2bf(unsigned a, unsigned b, float c) {
    return __builtin_amdgcn_fdot2_f32_bf16(__builtin_bit_cast(bf16x2, a), __builtin_bit_cast(bf16x2, b), c, false);
}
template <int BYTES> __device__ __forceinline__ unsigned code_prefetch(int t, bool on = true) {
    unsigned acc = 0;
    unsigned long long pc;
    asm volatile("s_getpc_b64 %0" : "=s"(pc));
    const char* base = (const char*)(pc & ~(unsigned long long)127);
    if (on && t * 128 < BYTES) acc = *(const volatile unsigned*)(base + t * 128);
    return acc;
}
#define CODE_PF_DONE(v) asm volatile("" :: "v"(v))
__device__ __forceinline__ f32x2 ln_stats(float mean, float var) {
    const float mb = rbf(mean);
    const float sd = rbf(__builtin_sqrtf(rbf(rbf(var) + 1e-5f)));
    return f32x2{mb, 1.0f / sd};
}

#define K0_PRIO 1
constexpr int K0_W1 = 128, K0_LN = 128, K0_KG = 512;
constexpr int K0_GRID = K0_W1 + K0_LN + K0_KG;
constexpr int K0_SMEM = 44 * 1024;
constexpr int K0_CODE = 11264, KM_CODE = 17920;

__device__ __forceinline__ void k0_w1(int b, const u16* __restrict__ wg, const u16* __restrict__ wu,
                                      u16* __restrict__ W1, char* smem) {
    const int nb = b >> 3, kp = b & 7, tid = threadIdx.x;
    u16* tile = (u16*)smem;
    u32x4 v[7];
#pragma unroll
    for (int i = 0; i < 7; ++i) {
        int c = tid + 256 * i;
        int gu = c / 896, rem = c - gu * 896, r = rem / 14, ch = rem - r * 14;
        v[i] = *(const u32x4*)((gu ? wu : wg) + (size_t)(64 * kp + r) * F_ + 112 * nb + ch * 8);
    }
    SCHED_FENCE();
#pragma unroll
    for (int i = 0; i < 7; ++i) {
        int c = tid + 256 * i;
        int gu = c / 896, rem = c - gu * 896, r = rem / 14, ch = rem - r * 14;
        *(u32x4*)(tile + (gu * 64 + r) * 120 + ch * 8) = v[i];
    }
    __syncthreads();
    u16* dst = W1 + (size_t)(nb * 16 + 2 * kp) * 14 * 512;
#pragma unroll
    for (int i = 0; i < 7; ++i) {
        int o = tid + 256 * i;
        int k16 = o / 448, rem = o - k16 * 448;
        int tl = rem >> 6, lane = rem & 63, fr = lane & 15, gu = (lane >> 4) & 1, h = lane >> 5;
        const u16* sp = tile + (gu * 64 + 16 * k16 + 8 * h) * 120 + 16 * tl + fr;
        u32x4 o4;
#pragma unroll
        for (int j = 0; j < 4; ++j) o4[j] = (unsigned)sp[(2 * j) * 120] | ((unsigned)sp[(2 * j + 1) * 120] << 16);
        gstore(dst + o * 8, o4);
    }
}

__device__ __forceinline__ void k0_w2_load(int b, const u16* __restrict__ wd, u32x4 (&v)[4]) {
    const int tid = threadIdx.x, ks = b >> 1, dh = b & 1;
#pragma unroll
    for (int i = 0; i < 4; ++i) {
        int c = tid + 256 * i, r = c >> 5, ch = c & 31;
        v[i] = *(const u32x4*)(wd + (size_t)(32 * ks + r) * D_ + 256 * dh + ch * 8);
    }
}
__device__ __forceinline__ void k0_w2_put(const u32x4 (&v)[4], char* smem) {
    const int tid = threadIdx.x;
    u16* tile = (u16*)smem;
#pragma unroll
    for (int i = 0; i < 4; ++i) {
        int c = tid + 256 * i, r = c >> 5, ch = c & 31;
        *(u32x4*)(tile + r * 264 + ch * 8) = v[i];
    }
}
__device__ __forceinline__ void k0_w2_store(int b, u16* __restrict__ W2, const char* smem) {
    const int tid = threadIdx.x, ks = b >> 1, dh = b & 1;
    const u16* tile = (const u16*)smem;
#pragma unroll
    for (int dbl = 0; dbl < 4; ++dbl) {
        int t = tid >> 6, lane = tid & 63, r = lane & 15, q = lane >> 4;
        int dl = 64 * dbl + 16 * (r >> 2) + 4 * t + (r & 3);
        const u16* sp = tile + (16 * (q >> 1) + 4 * (q & 1)) * 264 + dl;
        u32x4 o4;
        o4[0] = (unsigned)sp[0 * 264] | ((unsigned)sp[1 * 264] << 16);
        o4[1] = (unsigned)sp[2 * 264] | ((unsigned)sp[3 * 264] << 16);
        o4[2] = (unsigned)sp[8 * 264] | ((unsigned)sp[9 * 264] << 16);
        o4[3] = (unsigned)sp[10 * 264] | ((unsigned)sp[11 * 264] << 16);
        gstore(W2 + ((size_t)((4 * dh + dbl) * 56 + ks) * 4 + t) * 512 + lane * 8, o4);
    }
}

__device__ __forceinline__ void k0_ln(int b, const u16* __restrict__ x, u16* __restrict__ xnF, u16* __restrict__ xnT,
                                      f32x2* __restrict__ stat, const u16* __restrict__ wd, u16* __restrict__ W2, char* smem) {
    const int tid = threadIdx.x, w = tid >> 6, lane = tid & 63, rr = lane >> 4, c = lane & 15;
    constexpr int TS = 516;
    u16* tile = (u16*)smem;
    const int l0 = b * 16, rl = 4 * w + rr, row = l0 + rl;
    u32x4 raw[4], wv[4];
#pragma unroll
    for (int i = 0; i < 4; ++i) raw[i] = *(const u32x4*)(x + (size_t)row * D_ + (c + 16 * i) * 8);
    const int wb = b < 112 ? b : 111;
    k0_w2_load(wb, wd, wv);
    SCHED_FENCE();
    float v[32];
#pragma unroll
    for (int i = 0; i < 4; ++i)
#pragma unroll
        for (int j = 0; j < 4; ++j) { v[8 * i + 2 * j] = bflo(raw[i][j]); v[8 * i + 2 * j + 1] = bfhi(raw[i][j]); }
    float s = 0.f;
#pragma unroll
    for (int j = 0; j < 32; ++j) s += v[j];
    const float mean = row16_sum(s) * (1.0f / D_);
    float q2 = 0.f;
#pragma unroll
    for (int j = 0; j < 32; ++j) { float dlt = v[j] - mean; q2 += dlt * dlt; }
    const f32x2 st = ln_stats(mean, row16_sum(q2) * (1.0f / D_));
    if (c == 0) gstore(stat + row, st);
#pragma unroll
    for (int i = 0; i < 4; ++i) {
        u32x4 pb, ph;
#pragma unroll
        for (int j = 0; j < 4; ++j) {
            pb[j] = pack_bf(rbf(v[8 * i + 2 * j] - st[0]) * st[1], rbf(v[8 * i + 2 * j + 1] - st[0]) * st[1]);
            ph[j] = pack_h(bflo(pb[j]), bfhi(pb[j]));
        }
        const int ch = c + 16 * i;
        gstore(xnF + ((size_t)((row >> 5) * 32 + (ch >> 1)) * 64 + (ch & 1) * 32 + (row & 31)) * 8, pb);
        u16* tp = tile + rl * TS + ch * 8;
        *(u32x2*)(tp) = u32x2{ph[0], ph[1]};
        *(u32x2*)(tp + 4) = u32x2{ph[2], ph[3]};
    }
    k0_w2_put(wv, smem + 16896);
    __syncthreads();
    if (b < 112) k0_w2_store(b, W2, smem + 16896);
#pragma unroll
    for (int it = 0; it < 4; ++it) {
        int d = it * 128 + (tid >> 1), part = tid & 1;
        const u16* sp = tile + (8 * part) * TS + d;
        u32x4 o;
#pragma unroll
        for (int j = 0; j < 4; ++j) o[j] = (unsigned)sp[(2 * j) * TS] | ((unsigned)sp[(2 * j + 1) * TS] << 16);
        gstore(xnT + (size_t)d * L_ + l0 + 8 * part, o);
    }
}

__device__ __forceinline__ int bset_idx(int e) {
    if (e < 16) return e;
    int s = (31 - __builtin_clz(e)) - 3;
    return 8 + 8 * s + ((e - (8 << s)) >> s);
}
struct cplx { float r, i; };
__device__ __forceinline__ cplx cmul(cplx a, cplx b) { return cplx{a.r * b.r - a.i * b.i, a.r * b.i + a.i * b.r}; }
__device__ __forceinline__ cplx cexp_p(float zr2, float zi2, float p) {
    float mag = __builtin_amdgcn_exp2f(zr2 * p);
    float ph = __builtin_amdgcn_fractf(zi2 * p);
    return cplx{mag * __builtin_amdgcn_cosf(ph), mag * __builtin_amdgcn_sinf(ph)};
}

constexpr int CV_RS = 528;
__device__ __forceinline__ void k0_kgen(const int d, const u16* __restrict__ lar, const u16* __restrict__ aim,
                                        const u16* __restrict__ cre, const u16* __restrict__ cim,
                                        const u16* __restrict__ ldt, char* __restrict__ ws, char* smem) {
    const int tid = threadIdx.x, lane = tid & 63;
    const int w = __builtin_amdgcn_readfirstlane(tid >> 6);
    char* Ut = smem;
    char* Vt = smem + 41 * CV_RS;
    const int n = (w & 1) * 64 + lane;
    const u16 r_ldt = ldt[d];
    const u16 r_c0 = cre[d * NS_ + lane], r_c1 = cim[d * NS_ + lane], r_c2 = cre[d * NS_ + 64 + lane], r_c3 = cim[d * NS_ + 64 + lane];
    const u16 r_lar = lar[d * NS_ + n], r_aim = aim[d * NS_ + n];
    SCHED_FENCE();
    const float dt = rbf(__expf(bf2f(r_ldt)));
    float sabs;
    {
        const float rs = row16_sum(fabsf(bf2f(r_c0)) + fabsf(bf2f(r_c1)) + fabsf(bf2f(r_c2)) + fabsf(bf2f(r_c3)));
        const int ri = __builtin_bit_cast(int, rs);
        sabs = (__builtin_bit_cast(float, __builtin_amdgcn_readlane(ri, 0)) + __builtin_bit_cast(float, __builtin_amdgcn_readlane(ri, 16)) +
                __builtin_bit_cast(float, __builtin_amdgcn_readlane(ri, 32)) + __builtin_bit_cast(float, __builtin_amdgcn_readlane(ri, 48))) * dt;
    }
    int ex = (int)((__float_as_uint(sabs) >> 23) & 0xff);
    int se = 127 + 14 - (ex - 126);
    se = se < 1 ? 1 : (se > 254 ? 254 : se);
    const float sc = __uint_as_float((unsigned)se << 23);
    {
        const float ar = -rbf(__expf(bf2f(r_lar)));
        const float zr = ar * dt, zi = bf2f(r_aim) * dt;
        const bool isU = w >= 2;
        const float pb = isU ? 16.0f : 1.0f;
        const float zr2 = zr * 1.4426950408889634f;
        const float zi2 = zi * (isU ? -0.15915494309189535f : 0.15915494309189535f);
        cplx e[8];
#pragma unroll
        for (int k = 0; k < 8; ++k) e[k] = cexp_p(zr2, zi2, pb * (float)(1 << k));
        cplx v[41];
        {
            const float crv = bf2f((w & 1) ? r_c2 : r_c0), civ = bf2f((w & 1) ? r_c3 : r_c1);
            v[0] = isU ? cplx{crv * dt * sc, -(civ * dt * sc)} : cplx{1.0f, 0.0f};
        }
        char* tb = (isU ? Ut : Vt) + n * 4;
        auto put = [&](int i0, int i1) {
#pragma unroll
            for (int i = i0; i < i1; ++i) *(unsigned*)(tb + i * CV_RS) = pack_h(v[i].r, v[i].i);
            SCHED_FENCE();
        };
        v[1] = cmul(v[0], e[0]);
#pragma unroll
        for (int k = 0; k < 2; ++k) v[2 + k] = cmul(v[k], e[1]);
#pragma unroll
        for (int k = 0; k < 4; ++k) v[4 + k] = cmul(v[k], e[2]);
#pragma unroll
        for (int k = 0; k < 8; ++k) v[8 + k] = cmul(v[k], e[3]);
        v[40] = cmul(v[0], e[7]);
        if (isU) *(unsigned*)(tb + 40 * CV_RS) = pack_h(v[40].r, v[40].i);
        put(0, 16);
#pragma unroll
        for (int k = 0; k < 8; ++k) v[16 + k] = cmul(v[2 * k], e[4]);
        put(16, 24);
#pragma unroll
        for (int k = 0; k < 8; ++k) v[24 + k] = cmul(v[k < 4 ? 4 * k : 16 + (4 * k - 16) / 2], e[5]);
        put(24, 32);
#pragma unroll
        for (int k = 0; k < 8; ++k) {
            const int ee = 8 * k;
            const int src = ee < 16 ? ee : (ee < 32 ? 16 + (ee - 16) / 2 : 24 + (ee - 32) / 4);
            v[32 + k] = cmul(v[src], e[6]);
        }
        put(32, 40);
    }
    __syncthreads();
    const int j = lane & 15, q = lane >> 4;
    {
        const int urow = (w == 0) ? j : (8 + 8 * w + j);
        const int vrow = bset_idx(j << w);
        const char* ua = Ut + urow * CV_RS + q * 16;
        const char* vb = Vt + vrow * CV_RS + q * 16;
        f16x8 a[8], bb[8];
#pragma unroll
        for (int ks = 0; ks < 8; ++ks) { a[ks] = *(const f16x8*)(ua + ks * 64); bb[ks] = *(const f16x8*)(vb + ks * 64); }
        SCHED_FENCE();
        f32x4 acc0 = {0.f, 0.f, 0.f, 0.f}, acc1 = {0.f, 0.f, 0.f, 0.f};
#pragma unroll
        for (int ks = 0; ks < 8; ks += 2) {
            acc0 = __builtin_amdgcn_mfma_f32_16x16x32_f16(a[ks], bb[ks], acc0, 0, 0, 0);
            acc1 = __builtin_amdgcn_mfma_f32_16x16x32_f16(a[ks + 1], bb[ks + 1], acc1, 0, 0, 0);
        }
        u32x2 o = {pack_h(acc0[0] + acc1[0], acc0[1] + acc1[1]), pack_h(acc0[2] + acc1[2], acc0[3] + acc1[3])};
        gstore((u16*)(ws + WS_KD) + (size_t)d * 1024 + w * 256 + j * 16 + 4 * q, o);
        if (tid == 0) ((float*)(ws + WS_KSC))[d] = 2.0f / sc;
    }
}

__global__ __launch_bounds__(256) void k0_main(const u16* __restrict__ x, const u16* __restrict__ wg,
                                               const u16* __restrict__ wu, const u16* __restrict__ wd,
                                               const u16* __restrict__ lar, const u16* __restrict__ aim,
                                               const u16* __restrict__ cre, const u16* __restrict__ cim,
                                               const u16* __restrict__ ldt, char* __restrict__ ws) {
    __shared__ __attribute__((aligned(16))) char smem[K0_SMEM];
    const unsigned pf = code_prefetch<K0_CODE>(threadIdx.x);
    int b = blockIdx.x;
    STAMP(ws, 0, 0);
    if (b < K0_KG) {
        k0_kgen(b, lar, aim, cre, cim, ldt, ws, smem);
    } else if (b < K0_KG + K0_LN) {
        __builtin_amdgcn_s_setprio(K0_PRIO);
        k0_ln(b - K0_KG, x, (u16*)(ws + WS_XNF), (u16*)(ws + WS_XNT), (f32x2*)(ws + WS_STAT), wd, (u16*)(ws + WS_W2), smem);
    } else {
        __builtin_amdgcn_s_setprio(K0_PRIO);
        b -= K0_KG + K0_LN;
        if (b == 0) {
#pragma unroll
            for (int i = 0; i < 48; ++i) ((unsigned*)(ws + WS_FLAG))[threadIdx.x + 256 * i] = 0u;
        }
        k0_w1(b, wg, wu, (u16*)(ws + WS_W1), smem);
    }
    CODE_PF_DONE(pf);
    STAMP(ws, 0, 3);
}

#define CV_SLEEP 8
#define G_PRIO 2
#define CV_TRIG 1u
constexpr int KM_CV = 256, KM_G = 256;
constexpr int G1_STAGE = 22 * 1024;
constexpr int CV_COPY = 4160;
constexpr int CV_XOFF = 8 * CV_COPY;
constexpr int CV_KD = CV_XOFF + 4608;
constexpr int CV_CH = 39968;
constexpr int KM_SMEM = 2 * CV_CH;

__device__ __forceinline__ float gelu_tanh(float x) {
    const float t = x * (-2.302208198f - 0.1029432397f * x * x);
    return x * __builtin_amdgcn_rcpf(1.0f + __builtin_amdgcn_exp2f(t));
}
__device__ __forceinline__ void glds16(unsigned ldsaddr, unsigned voff, const void* sbase) {
    asm volatile("s_mov_b32 m0, %0\n\ts_nop 0\n\tglobal_load_lds_dwordx4 %1, %2"
                 :: "s"(ldsaddr), "v"(voff), "s"(sbase) : "memory");
}
#define GLDS_OFF(voff, sbase, off) asm volatile("global_load_lds_dwordx4 %0, %1 offset:%2" :: "v"(voff), "s"(sbase), "i"(off) : "memory")
#define DS_RD128(dst, addr, off) asm volatile("ds_read_b128 %0, %1 offset:%2" : "=v"(dst) : "v"(addr), "i"(off))
typedef __attribute__((ext_vector_type(16))) float f32x16;

template <int NT>
__device__ __forceinline__ void km_gemm1_wave(const int w, const int lane, const int mb, const int nb, char* smem, char* ws) {
    const int lg = w & 3;
    constexpr int tl0 = NT == 4 ? 0 : 4;
    const unsigned lds0 = (unsigned)(uintptr_t)(LAS char*)smem;
    const int wfr = NT == 4 ? 3 * lg : (lg == 0 ? 12 : 13);
    const char* wbase = ws + WS_W1 + ((size_t)nb * 16 * 14 + wfr) * 1024;
    const char* xbase = ws + WS_XNF + ((size_t)(mb * 4 + lg) * 16) * 2048;
    const unsigned l16 = lane * 16;
    const unsigned wdst = lds0 + wfr * 1024, xdst = lds0 + (14 + 2 * lg) * 1024;
    auto stage = [&](int ks, unsigned boff) {
        const char* wb = wbase + ks * 14336;
        glds16(wdst + boff, l16, wb);
        if constexpr (NT == 4) {
            GLDS_OFF(l16, wb, 1024);
            GLDS_OFF(l16, wb, 2048);
        } else {
            const char* xb = xbase + ks * 2048;
            glds16(xdst + boff, l16, xb);
            GLDS_OFF(l16, xb, 1024);
        }
    };
    const unsigned aw = lds0 + tl0 * 1024 + l16, ax = lds0 + (14 + 2 * lg) * 1024 + l16;
    f32x16 acc[NT];
#pragma unroll
    for (int t = 0; t < NT; ++t)
#pragma unroll
        for (int i = 0; i < 16; ++i) acc[t][i] = 0.f;
    u32x4 wf[2][NT], xf[2];
    stage(0, 0);
    stage(1, G1_STAGE);
    stage(2, 2 * G1_STAGE);
    asm volatile("s_waitcnt vmcnt(6)" ::: "memory");
    __builtin_amdgcn_s_barrier();
    SCHED_FENCE();
    STAMP(ws, 1, 1);
    DS_RD128(xf[0], ax, 0);
#pragma unroll
    for (int t = 0; t < NT; ++t) DS_RD128(wf[0][t], aw, t * 1024);
    DS_RD128(xf[1], ax, 1024);
#pragma unroll
    for (int t = 0; t < NT; ++t) DS_RD128(wf[1][t], aw, 7168 + t * 1024);
    SCHED_FENCE();
    auto iter = [&](int ks, auto bufc, auto modec) {
        constexpr int BUF = decltype(bufc)::value;
        constexpr int MODE = decltype(modec)::value;
        constexpr int NB = ((BUF + 1) % 3) * G1_STAGE;
#pragma unroll
        for (int t = 0; t < NT; ++t) {
            if (t == 0) asm volatile("s_waitcnt lgkmcnt(%3)" : "+v"(wf[0][0]), "+v"(wf[0][1]), "+v"(xf[0]) : "i"(2 * NT - 1));
            if (t == 2) {
                if constexpr (NT == 4) asm volatile("s_waitcnt lgkmcnt(%2)" : "+v"(wf[0][2]), "+v"(wf[0][NT - 1]) : "i"(NT + 1));
                else asm volatile("s_waitcnt lgkmcnt(%1)" : "+v"(wf[0][2]) : "i"(NT + 1));
            }
            acc[t] = __builtin_amdgcn_mfma_f32_32x32x16_bf16(__builtin_bit_cast(bf16x8, wf[0][t]), __builtin_bit_cast(bf16x8, xf[0]), acc[t], 0, 0, 0);
            SCHED_FENCE();
        }
        if constexpr (NT == 4) asm volatile("s_waitcnt lgkmcnt(0)" : "+v"(xf[1]), "+v"(wf[1][0]), "+v"(wf[1][1]), "+v"(wf[1][2]), "+v"(wf[1][NT - 1]) :: "memory");
        else asm volatile("s_waitcnt lgkmcnt(0)" : "+v"(xf[1]), "+v"(wf[1][0]), "+v"(wf[1][1]), "+v"(wf[1][2]) :: "memory");
        if (MODE < 3) {
            if (MODE < 2) asm volatile("s_waitcnt vmcnt(3)" ::: "memory");
            else asm volatile("s_waitcnt vmcnt(0)" ::: "memory");
            SCHED_FENCE();
            __builtin_amdgcn_s_barrier();
        }
        SCHED_FENCE();
#pragma unroll
        for (int t = 0; t < NT; ++t) {
            acc[t] = __builtin_amdgcn_mfma_f32_32x32x16_bf16(__builtin_bit_cast(bf16x8, wf[1][t]), __builtin_bit_cast(bf16x8, xf[1]), acc[t], 0, 0, 0);
            SCHED_FENCE();
            if (MODE < 3 && t == 0) {
                DS_RD128(xf[0], ax, NB);
#pragma unroll
                for (int u = 0; u < NT; ++u) DS_RD128(wf[0][u], aw, NB + u * 1024);
            }
            if (MODE == 0 && t == 1) stage(ks + 3, BUF * G1_STAGE);
            SCHED_FENCE();
        }
        if (MODE < 3) {
            DS_RD128(xf[1], ax, NB + 1024);
#pragma unroll
            for (int t = 0; t < NT; ++t) DS_RD128(wf[1][t], aw, NB + 7168 + t * 1024);
        }
        SCHED_FENCE();
    };
    using I0 = std::integral_constant<int, 0>; using I1 = std::integral_constant<int, 1>;
    using I2 = std::integral_constant<int, 2>; using I3 = std::integral_constant<int, 3>;
#pragma unroll 1
    for (int k3 = 0; k3 < 12; k3 += 3) {
        iter(k3, I0{}, I0{});
        iter(k3 + 1, I1{}, I0{});
        iter(k3 + 2, I2{}, I0{});
    }
    iter(12, I0{}, I0{});
    iter(13, I1{}, I1{});
    iter(14, I2{}, I2{});
    iter(15, I0{}, I3{});
    __builtin_amdgcn_s_barrier();
    if (NT == 4 && w == 0 && lane < 4)
        __hip_atomic_fetch_add((GAS unsigned*)(ws + WS_FLAG) + 64 * (64 + 8 * mb + 4 + lane), 1u, __ATOMIC_RELAXED, __HIP_MEMORY_SCOPE_AGENT);
    STAMP(ws, 1, 2);
    const int hh = lane >> 5;
    const int lt = 8 * mb + 2 * lg + ((lane >> 4) & 1);
#pragma unroll
    for (int t = 0; t < NT; ++t) {
        const int T = 7 * nb + tl0 + t;
        u32x4 p;
#pragma unroll
        for (int i2 = 0; i2 < 4; ++i2) {
            float hv[2];
#pragma unroll
            for (int r = 0; r < 2; ++r) {
                const float g = rbf(acc[t][2 * i2 + r]), u = rbf(acc[t][8 + 2 * i2 + r]);
                hv[r] = rbf(gelu_tanh(g)) * u;
            }
            p[i2] = pack_bf(hv[0], hv[1]);
        }
        gstore(ws + WS_HF + ((size_t)(lt * 56 + (T >> 1)) * 64 + (2 * (T & 1) + hh) * 16 + (lane & 15)) * 16, p);
    }
}

struct IdxTab { u16 v[2064]; };
constexpr IdxTab make_idx_tab() {
    IdxTab t{};
    for (int i = 0; i < 2064; ++i) {
        int lag = 2047 - i;
        if (lag < 0) { t.v[i] = 1024; continue; }
        int p = lag;
        if (lag >= 256) {
            int s = 0;
            while ((lag >> s) >= 256) ++s;
            int step = 1 << s, rem = lag & (step - 1), base = lag - rem, half = step >> 1;
            if (rem > half || (rem == half && ((base >> s) & 1))) base += step;
            p = base;
        }
        int e = 0, r = p;
        if (p >= 256) {
            while ((p >> e) >= 256) ++e;
            if (e > 3) e = 3;
            r = (p - (128 << e)) >> e;
        }
        t.v[i] = (u16)(e * 256 + (r & 15) * 16 + (r >> 4));
    }
    return t;
}
__device__ const IdxTab kIdxTab = make_idx_tab();

__device__ __forceinline__ void km_conv(const int cb, char* __restrict__ ws, char* smem0) {
    const int tid = threadIdx.x & 255, lane = tid & 63;
    const int w8 = __builtin_amdgcn_readfirstlane(threadIdx.x >> 6);
    const int ch = w8 >> 2, w = w8 & 3;
    const int d = 2 * cb + ch;
    char* smem = smem0 + ch * CV_CH;
    u16* Kd = (u16*)(smem + CV_KD);
    const u32x2 kdv = *(const u32x2*)((const u16*)(ws + WS_KD) + (size_t)d * 1024 + tid * 4);
    const float osc = ((const float*)(ws + WS_KSC))[d];
    const u32x4 it0 = *(const u32x4*)(kIdxTab.v + 8 * tid), it1 = *(const u32x4*)(kIdxTab.v + 8 * tid + 8);
    const u32x4 xrow = *(const u32x4*)((const u16*)(ws + WS_XNT) + (size_t)d * L_ + tid * 8);
    SCHED_FENCE();
    *(u32x2*)(Kd + tid * 4) = kdv;
    if (tid == 0) Kd[1024] = 0;
    if (w8 == 0) {
        GAS unsigned* cnt = (GAS unsigned*)(ws + WS_FLAG) + 64 * (64 + 8 * (cb & 15) + 4 + ((cb >> 4) & 3));
        for (unsigned spins = 0; spins < 128; ++spins) {
            if (__builtin_amdgcn_readfirstlane(__hip_atomic_load(cnt, __ATOMIC_RELAXED, __HIP_MEMORY_SCOPE_AGENT)) >= CV_TRIG) break;
            __builtin_amdgcn_s_sleep(CV_SLEEP);
        }
    }
    __syncthreads();
    STAMP(ws, 1, 1);
    const int j = lane & 15, q = lane >> 4;
    {
        unsigned wd[8];
#pragma unroll
        for (int e = 0; e < 4; ++e) {
            wd[e] = (unsigned)Kd[it0[e] & 0xffff] | ((unsigned)Kd[it0[e] >> 16] << 16);
            wd[4 + e] = (unsigned)Kd[it1[e] & 0xffff] | ((unsigned)Kd[it1[e] >> 16] << 16);
        }
        char* dst = smem + tid * 16;
#pragma unroll
        for (int e = 0; e < 4; ++e) {
            u32x4 ev = {wd[e], wd[e + 1], wd[e + 2], wd[e + 3]};
            *(u32x4*)(dst + (2 * e) * CV_COPY) = ev;
            u32x4 od;
#pragma unroll
            for (int k = 0; k < 4; ++k) od[k] = __builtin_amdgcn_alignbit(wd[e + k + 1], wd[e + k], 16);
            *(u32x4*)(dst + (2 * e + 1) * CV_COPY) = od;
        }
        char* xp = smem + CV_XOFF;
        if (tid < 32) *(u32x4*)(xp + tid * 16) = u32x4{0u, 0u, 0u, 0u};
        if (tid >= 64 && tid < 72) *(u32x4*)(smem + (tid - 64) * CV_COPY + 256 * 16) = u32x4{0u, 0u, 0u, 0u};
        *(u32x4*)(xp + 512 + tid * 16) = xrow;
    }
    __syncthreads();
    STAMP(ws, 1, 2);
    const char* kb = smem + ((7 - j) & 7) * CV_COPY + (253 + q - (j >> 3)) * 16 - w * 2 * 64;
    const char* xq = smem + CV_XOFF + (256 + 16 * j - 16 + 8 * q) * 2 - w * 2 * 64;
    f32x4 acc[8];
#pragma unroll
    for (int i = 0; i < 8; ++i) acc[i] = f32x4{0.f, 0.f, 0.f, 0.f};
    {
        f16x8 bfr[2][8], af[3];
#pragma unroll
        for (int c = 0; c < 8; ++c) bfr[0][c] = *(const f16x8*)(xq + c * 512);
        af[0] = *(const f16x8*)(kb);
        af[1] = *(const f16x8*)(kb - 8 * 64);
        SCHED_FENCE();
#pragma unroll
        for (int ri = 0; ri < 2; ++ri) {
#pragma unroll
            for (int mm = 0; mm < 8; ++mm) {
                const int g = ri * 8 + mm;
                if (g + 2 < 16) {
                    const int g2 = g + 2;
                    af[g2 % 3] = *(const f16x8*)(kb - (8 * (g2 & 7) + (g2 >> 3)) * 64);
                }
                if (ri < 1) bfr[1][mm] = *(const f16x8*)(xq + mm * 512 - 64);
#pragma unroll
                for (int ct = mm; ct < 8; ++ct)
                    acc[ct] = __builtin_amdgcn_mfma_f32_16x16x32_f16(af[g % 3], bfr[ri][ct - mm], acc[ct], 0, 0, 0);
                SCHED_FENCE();
            }
        }
    }
    STAMP(ws, 1, 4);
    __syncthreads();
    {
        f32x4* exb = (f32x4*)(smem + w * 8192) + lane;
#pragma unroll
        for (int i = 0; i < 8; ++i) exb[i * 64] = acc[i];
    }
    __syncthreads();
    {
        u16* yp = (u16*)(ws + WS_YT) + (size_t)d * L_ + 16 * j + 4 * q + 512 * w;
#pragma unroll
        for (int i = 0; i < 2; ++i) {
            const f32x4* exb = (const f32x4*)(smem + (2 * w + i) * 1024) + lane;
            f32x4 o = exb[0] + exb[512] + exb[1024] + exb[1536];
            u32x2 p = {pack_bf(o[0] * osc, o[1] * osc), pack_bf(o[2] * osc, o[3] * osc)};
            gstore(yp + 256 * i, p);
        }
    }
    STAMP(ws, 1, 6);
    asm volatile("s_waitcnt vmcnt(0)" ::: "memory");
    __syncthreads();
    if (threadIdx.x < 8) __hip_atomic_fetch_add((GAS unsigned*)(ws + WS_FLAG) + 64 * (8 * (cb >> 5) + threadIdx.x), 1u, __ATOMIC_RELAXED, __HIP_MEMORY_SCOPE_AGENT);
}

struct G2Map { int lb, db; };
__device__ __forceinline__ G2Map g2_map(int bid) { const int xcd = bid & 7, i5 = bid >> 3; return G2Map{4 * xcd + (i5 & 3), i5 >> 2}; }
__device__ __forceinline__ void g2_stage_w(const int bid, const int w8, const int lane, const int k, char* ws, char* smem) {
    const int kg = w8 & 3, lh = w8 >> 2;
    const unsigned lds0 = (unsigned)(uintptr_t)(LAS char*)smem;
    const char* src = ws + WS_W2 + ((size_t)(g2_map(bid).db * 56 + 14 * kg + k) * 4 + 2 * lh) * 1024;
    const unsigned l16 = lane * 16;
    glds16(lds0 + (k & 3) * 16384 + kg * 4096 + lh * 2048, l16, src);
    GLDS_OFF(l16, src, 1024);
}
#define HLOAD(dst, voff, base, off) asm volatile("global_load_dwordx4 %0, %1, %2 offset:%3 sc1" : "=v"(dst) : "v"(voff), "s"(base), "i"(off))
__device__ __forceinline__ void km_gemm2(const int bid, const int w8, const int lane, const u16* __restrict__ x,
                                         const u16* __restrict__ dskip, char* __restrict__ ws, u16* __restrict__ out, char* smem) {
    const int kg = w8 & 3, lh = w8 >> 2;
    const int xcd = bid & 7;
    const int lb = g2_map(bid).lb, db = g2_map(bid).db;
    const int r = lane & 15, q = lane >> 4;
    const __amdgpu_buffer_rsrc_t yrs = __builtin_amdgcn_make_buffer_rsrc(ws + WS_YT, 0, D_ * L_ * 2, 0x00020000);
    const int l16 = lane * 16;
    const char* hb0 = ws + WS_HF + (size_t)((4 * lb + 2 * lh) * 56 + 14 * kg) * 1024;
    const char* hb1 = hb0 + 56 * 1024;
    const unsigned awr = (unsigned)(uintptr_t)(LAS char*)smem + kg * 4096 + l16;
    if (w8 == 0) {
        GAS unsigned* cnt = (GAS unsigned*)(ws + WS_FLAG) + 64 * (64 + 8 * (lb >> 1) + (db & 3));
        for (unsigned spins = 0; spins < (1u << 22); ++spins) {
            if (__builtin_amdgcn_readfirstlane(__hip_atomic_load(cnt, __ATOMIC_RELAXED, __HIP_MEMORY_SCOPE_AGENT)) == 16u) break;
            __builtin_amdgcn_s_sleep(4);
        }
    }
    __syncthreads();
    STAMP(ws, 1, 5);
    u32x4 hf[3][2], wf[4];
#pragma unroll
    for (int s = 0; s < 3; ++s) { HLOAD(hf[s][0], l16, hb0, s * 1024); HLOAD(hf[s][1], l16, hb1, s * 1024); }
    SCHED_FENCE();
    f32x4 acc[4][2];
#pragma unroll
    for (int t = 0; t < 4; ++t)
#pragma unroll
        for (int i = 0; i < 2; ++i) acc[t][i] = f32x4{0.f, 0.f, 0.f, 0.f};
    const int oi = kg >> 1, ot = 2 * (kg & 1);
    const int lrow = 64 * lb + 32 * lh + 16 * oi + r;
    const int dcol = 64 * db + 16 * q + 4 * ot;
    auto poll = [&]() {
        return __hip_atomic_load((GAS unsigned*)(ws + WS_FLAG) + 64 * (8 * db + xcd), __ATOMIC_RELAXED, __HIP_MEMORY_SCOPE_AGENT);
    };
    auto ldy = [&]() {
        int ln = l16; asm volatile("" : "+v"(ln));
        const int row = ln >> 5;
        const int yo = (16 * (row >> 3) + (row & 7)) * (L_ * 2) + (ln & 16);
        const int so = ((64 * db + 4 * ot) * L_ + 64 * lb + 32 * lh + 16 * oi) * 2;
        return __builtin_amdgcn_raw_buffer_load_b128(yrs, yo, so, 16);
    };
    unsigned fv = 0;
    bool early = false;
    u32x4 yv, xv, dv;
    f32x2 st;
#pragma unroll
    for (int k = 0; k < 14; ++k) {
        const int s = k % 3;
        constexpr int kN[14] = {4, 6, 8, 8, 8, 8, 8, 8, 8, 8, 8, 8, 4, 0};
        asm volatile("s_waitcnt vmcnt(%2)" : "+v"(hf[s][0]), "+v"(hf[s][1]) : "i"(kN[k]) : "memory");
        __builtin_amdgcn_s_barrier();
        SCHED_FENCE();
#pragma unroll
        for (int t = 0; t < 4; ++t) DS_RD128(wf[t], awr, (k & 3) * 16384 + t * 1024);
#pragma unroll
        for (int t = 0; t < 4; ++t) {
            asm volatile("s_waitcnt lgkmcnt(%1)" : "+v"(wf[t]) : "i"(3 - t));
#pragma unroll
            for (int i = 0; i < 2; ++i)
                acc[t][i] = __builtin_amdgcn_mfma_f32_16x16x32_bf16(__builtin_bit_cast(bf16x8, wf[t]), __builtin_bit_cast(bf16x8, hf[s][i]), acc[t][i], 0, 0, 0);
            SCHED_FENCE();
        }
        if (k + 3 < 14) {
            g2_stage_w(bid, w8, lane, k + 3, ws, smem);
            const char* h0 = hb0 + (k + 3) * 1024;
            const char* h1 = hb1 + (k + 3) * 1024;
            HLOAD(hf[s][0], l16, h0, 0);
            HLOAD(hf[s][1], l16, h1, 0);
        }
        if (k == 11) fv = poll();
        if (k == 13) {
            early = __builtin_amdgcn_readfirstlane(fv) == 32u;
            if (early) yv = ldy();
            xv = *(const u32x4*)(x + (size_t)lrow * D_ + dcol);
            dv = *(const u32x4*)(dskip + dcol);
            st = ((const f32x2*)(ws + WS_STAT))[lrow];
        }
        SCHED_FENCE();
    }
    __builtin_amdgcn_s_barrier();
    STAMP(ws, 1, 6);
    {
        f32x4* red = (f32x4*)smem + lh * (24 * 64);
#pragma unroll
        for (int dk = 0; dk < 4; ++dk) {
            if (dk != kg) {
                const int si = kg < dk ? kg : kg - 1;
#pragma unroll
                for (int tt = 0; tt < 2; ++tt) red[((dk * 3 + si) * 2 + tt) * 64 + lane] = acc[2 * (dk & 1) + tt][dk >> 1];
            }
        }
    }
    if (w8 == 0 && !early) {
        for (unsigned spins = 0; spins < (1u << 22); ++spins) {
            if (__builtin_amdgcn_readfirstlane(poll()) == 32u) break;
            __builtin_amdgcn_s_sleep(8);
        }
    }
    __syncthreads();
    if (!early) yv = ldy();
    STAMP(ws, 1, 7);
    f32x4 f[2];
    {
        const f32x4* red = (const f32x4*)smem + lh * (24 * 64);
#pragma unroll
        for (int tt = 0; tt < 2; ++tt)
            f[tt] = kg == 0 ? acc[tt][0] : (kg == 1 ? acc[2 + tt][0] : (kg == 2 ? acc[tt][1] : acc[2 + tt][1]));
#pragma unroll
        for (int si = 0; si < 3; ++si)
#pragma unroll
            for (int tt = 0; tt < 2; ++tt) f[tt] += red[((kg * 3 + si) * 2 + tt) * 64 + lane];
    }
    u16 yt[8];
    {
        char* yb = smem + lh * 24576 + kg * 6144;
        asm volatile("" ::: "memory");
        *(u32x4*)(yb + lane * 16) = yv;
        asm volatile("s_waitcnt lgkmcnt(0)" ::: "memory");
        const u16* yr = (const u16*)(yb + q * 256 + 2 * r);
#pragma unroll
        for (int e = 0; e < 8; ++e) yt[e] = yr[e * 16];
    }
    u32x4 ov;
#pragma unroll
    for (int jj = 0; jj < 4; ++jj) {
        float res[2];
#pragma unroll
        for (int hh = 0; hh < 2; ++hh) {
            const int e = 2 * jj + hh;
            const float fv2 = rbf(f[e >> 2][e & 3]);
            const float g = rbf(fv2 * __builtin_amdgcn_rcpf(1.0f + __expf(-fv2)));
            const float ds = hh ? bfhi(dv[jj]) : bflo(dv[jj]);
            const float xx = hh ? bfhi(xv[jj]) : bflo(xv[jj]);
            const float xn = rbf(rbf(xx - st[0]) * st[1]);
            const float s = rbf(bf2f(yt[e]) + rbf(xn * ds));
            res[hh] = xx + rbf(s * g);
        }
        ov[jj] = pack_bf(res[0], res[1]);
    }
    *(u32x4*)(out + (size_t)lrow * D_ + dcol) = ov;
}

__global__ __launch_bounds__(512, 4) void km_main(const u16* __restrict__ x, const u16* __restrict__ dskip,
                                                  char* __restrict__ ws, u16* __restrict__ out) {
    __shared__ __attribute__((aligned(16))) char smem[KM_SMEM];
    const int b = blockIdx.x;
    const unsigned pf = code_prefetch<KM_CODE>(threadIdx.x, b >= KM_G);
    STAMP(ws, 1, 0);
    if (b < KM_G) {
        const int lane = threadIdx.x & 63;
        const int w = __builtin_amdgcn_readfirstlane(threadIdx.x >> 6);
        const int xcd = b & 7, i5 = b >> 3;
        const int mb = (xcd & 1) * 8 + (i5 & 7), nb = (xcd >> 1) * 4 + (i5 >> 3);
        __builtin_amdgcn_s_setprio(G_PRIO);
        if (w < 4) km_gemm1_wave<4>(w, lane, mb, nb, smem, ws);
        else km_gemm1_wave<3>(w, lane, mb, nb, smem, ws);
#pragma unroll
        for (int k = 0; k < 3; ++k) g2_stage_w(b, w, lane, k, ws, smem);
        asm volatile("s_waitcnt vmcnt(6)" ::: "memory");
        __builtin_amdgcn_s_barrier();
        if (threadIdx.x < 4) __hip_atomic_fetch_add((GAS unsigned*)(ws + WS_FLAG) + 64 * (64 + 8 * mb + threadIdx.x), 1u, __ATOMIC_RELAXED, __HIP_MEMORY_SCOPE_AGENT);
        STAMP(ws, 1, 4);
        km_gemm2(b, w, lane, x, dskip, ws, out, smem);
    } else {
        km_conv(b - KM_G, ws, smem);
        CODE_PF_DONE(pf);
    }
    STAMP(ws, 1, 3);
}

extern "C" void kernel_launch(void* const* d_in, const int* in_sizes, int n_in,
                              void* d_out, int out_size, void* d_ws, size_t ws_size,
                              hipStream_t stream) {
    const u16* x   = (const u16*)d_in[0];
    const u16* lar = (const u16*)d_in[1];
    const u16* aim = (const u16*)d_in[2];
    const u16* cre = (const u16*)d_in[3];
    const u16* cim = (const u16*)d_in[4];
    const u16* ldt = (const u16*)d_in[5];
    const u16* dsk = (const u16*)d_in[6];
    const u16* wg  = (const u16*)d_in[8];
    const u16* wu  = (const u16*)d_in[9];
    const u16* wd  = (const u16*)d_in[10];
    char* ws = (char*)d_ws;
    k0_main<<<K0_GRID, 256, 0, stream>>>(x, wg, wu, wd, lar, aim, cre, cim, ldt, ws);
    km_main<<<KM_G + KM_CV, 512, 0, stream>>>(x, dsk, ws, (u16*)d_out);
}
```

```cpp
#include <hip/hip_runtime.h>
#include <hip/hip_bf16.h>
#include <stdint.h>
#include <type_traits>

typedef __attribute__((ext_vector_type(8))) __bf16 bf16x8;
typedef __attribute__((ext_vector_type(2))) __bf16 bf16x2;
typedef __attribute__((ext_vector_type(8))) _Float16 f16x8;
typedef __attribute__((ext_vector_type(4))) float f32x4;
typedef __attribute__((ext_vector_type(2))) float f32x2;
typedef __attribute__((ext_vector_type(4))) unsigned int u32x4;
typedef __attribute__((ext_vector_type(2))) unsigned int u32x2;
typedef unsigned short u16;
#define GAS __attribute__((address_space(1)))
#define LAS __attribute__((address_space(3)))

constexpr int L_ = 2048, D_ = 512, NS_ = 128, F_ = 1792;

constexpr size_t WS_XNT  = 0;
constexpr size_t WS_STAT = 2u << 20;
constexpr size_t WS_FLAG = (2u << 20) + 65536;
constexpr size_t WS_W2   = 3u << 20;
constexpr size_t WS_HF   = 5u << 20;
constexpr size_t WS_YT   = 13u << 20;
constexpr size_t WS_KD   = 15u << 20;
constexpr size_t WS_KSC  = 17u << 20;
constexpr size_t WS_XNF  = 18u << 20;
constexpr size_t WS_W1   = 20u << 20;

#define STAMP(ws_, kid, slot) do {} while (0)
#define SCHED_FENCE() __builtin_amdgcn_sched_barrier(0)

template <typename T> __device__ __forceinline__ void gstore(void* p, T v) {
    if constexpr (sizeof(T) == 16) asm volatile("global_store_dwordx4 %0, %1, off sc0 sc1\n\ts_nop 1" :: "v"(p), "v"(v));
    else asm volatile("global_store_dwordx2 %0, %1, off sc0 sc1\n\ts_nop 1" :: "v"(p), "v"(v));
}
__device__ __forceinline__ float bf2f(u16 v) { return __uint_as_float(((unsigned)v) << 16); }
__device__ __forceinline__ float bflo(unsigned v) { return __uint_as_float(v << 16); }
__device__ __forceinline__ float bfhi(unsigned v) { return __uint_as_float(v & 0xffff0000u); }
__device__ __forceinline__ u16 f2bf(float f) { __bf16 b = (__bf16)f; return __builtin_bit_cast(u16, b); }
__device__ __forceinline__ float rbf(float f) { return bf2f(f2bf(f)); }
__device__ __forceinline__ unsigned pack_bf(float lo, float hi) {
    return (unsigned)f2bf(lo) | ((unsigned)f2bf(hi) << 16);
}
__device__ __forceinline__ u16 f2h(float f) { _Float16 h = (_Float16)f; return __builtin_bit_cast(u16, h); }
typedef __attribute__((ext_vector_type(2))) _Float16 f16x2;
__device__ __forceinline__ unsigned pack_h(float lo, float hi) {
    return (unsigned)f2h(lo) | ((unsigned)f2h(hi) << 16);
}
__device__ __forceinline__ float wave_sum(float v) {
#pragma unroll
    for (int o = 32; o > 0; o >>= 1) v += __shfl_xor(v, o, 64);
    return v;
}
__device__ __forceinline__ float row16_sum(float v) {
    v += __builtin_bit_cast(float, __builtin_amdgcn_update_dpp(0, __builtin_bit_cast(int, v), 0x128, 0xf, 0xf, false));
    v += __builtin_bit_cast(float, __builtin_amdgcn_update_dpp(0, __builtin_bit_cast(int, v), 0x124, 0xf, 0xf, false));
    v += __builtin_bit_cast(float, __builtin_amdgcn_update_dpp(0, __builtin_bit_cast(int, v), 0x122, 0xf, 0xf, false));
    v += __builtin_bit_cast(float, __builtin_amdgcn_update_dpp(0, __builtin_bit_cast(int, v), 0x121, 0xf, 0xf, false));
    return v;
}
__device__ __forceinline__ float dot2bf(unsigned a, unsigned b, float c) {
    return __builtin_amdgcn_fdot2_f32_bf16(__builtin_bit_cast(bf16x2, a), __builtin_bit_cast(bf16x2, b), c, false);
}
template <int BYTES> __device__ __forceinline__ unsigned code_prefetch(int t, bool on = true) {
    unsigned acc = 0;
    unsigned long long pc;
    asm volatile("s_getpc_b64 %0" : "=s"(pc));
    const char* base = (const char*)(pc & ~(unsigned long long)127);
    if (on && t * 128 < BYTES) acc = *(const volatile unsigned*)(base + t * 128);
    return acc;
}
#define CODE_PF_DONE(v) asm volatile("" :: "v"(v))
__device__ __forceinline__ f32x2 ln_stats(float mean, float var) {
    const float mb = rbf(mean);
    const float sd = rbf(__builtin_sqrtf(rbf(rbf(var) + 1e-5f)));
    return f32x2{mb, 1.0f / sd};
}

#define K0_PRIO 1
constexpr int K0_W1 = 128, K0_LN = 128, K0_KG = 512;
constexpr int K0_GRID = K0_W1 + K0_LN + K0_KG;
constexpr int K0_SMEM = 44 * 1024;
constexpr int K0_CODE = 11264, KM_CODE = 17920;

__device__ __forceinline__ void k0_w2_load(int b, const u16* __restrict__ wd, u32x4 (&v)[4]) {
    const int tid = threadIdx.x, ks = b >> 1, dh = b & 1;
#pragma unroll
    for (int i = 0; i < 4; ++i) {
        int c = tid + 256 * i, r = c >> 5, ch = c & 31;
        v[i] = *(const u32x4*)(wd + (size_t)(32 * ks + r) * D_ + 256 * dh + ch * 8);
    }
}
__device__ __forceinline__ void k0_w2_put(const u32x4 (&v)[4], char* smem) {
    const int tid = threadIdx.x;
    u16* tile = (u16*)smem;
#pragma unroll
    for (int i = 0; i < 4; ++i) {
        int c = tid + 256 * i, r = c >> 5, ch = c & 31;
        *(u32x4*)(tile + r * 264 + ch * 8) = v[i];
    }
}
__device__ __forceinline__ void k0_w2_store(int b, u16* __restrict__ W2, const char* smem) {
    const int tid = threadIdx.x, ks = b >> 1, dh = b & 1;
    const u16* tile = (const u16*)smem;
#pragma unroll
    for (int dbl = 0; dbl < 4; ++dbl) {
        int t = tid >> 6, lane = tid & 63, r = lane & 15, q = lane >> 4;
        int dl = 64 * dbl + 16 * (r >> 2) + 4 * t + (r & 3);
        const u16* sp = tile + (16 * (q >> 1) + 4 * (q & 1)) * 264 + dl;
        u32x4 o4;
        o4[0] = (unsigned)sp[0 * 264] | ((unsigned)sp[1 * 264] << 16);
        o4[1] = (unsigned)sp[2 * 264] | ((unsigned)sp[3 * 264] << 16);
        o4[2] = (unsigned)sp[8 * 264] | ((unsigned)sp[9 * 264] << 16);
        o4[3] = (unsigned)sp[10 * 264] | ((unsigned)sp[11 * 264] << 16);
        gstore(W2 + ((size_t)((4 * dh + dbl) * 56 + ks) * 4 + t) * 512 + lane * 8, o4);
    }
}

__device__ __forceinline__ void k0_w1(int b, const u16* __restrict__ wg, const u16* __restrict__ wu,
                                      u16* __restrict__ W1, const u16* __restrict__ wd, u16* __restrict__ W2, char* smem) {
    const int nb = b >> 3, kp = b & 7, tid = threadIdx.x;
    u16* tile = (u16*)smem;
    u32x4 v[7];
#pragma unroll
    for (int i = 0; i < 7; ++i) {
        int c = tid + 256 * i;
        int gu = c / 896, rem = c - gu * 896, r = rem / 14, ch = rem - r * 14;
        v[i] = *(const u32x4*)((gu ? wu : wg) + (size_t)(64 * kp + r) * F_ + 112 * nb + ch * 8);
    }
    u32x4 wv[4];
    k0_w2_load(b < 112 ? b : 111, wd, wv);
    SCHED_FENCE();
#pragma unroll
    for (int i = 0; i < 7; ++i) {
        int c = tid + 256 * i;
        int gu = c / 896, rem = c - gu * 896, r = rem / 14, ch = rem - r * 14;
        *(u32x4*)(tile + (gu * 64 + r) * 120 + ch * 8) = v[i];
    }
    __syncthreads();
    u16* dst = W1 + (size_t)(nb * 16 + 2 * kp) * 14 * 512;
#pragma unroll
    for (int i = 0; i < 7; ++i) {
        int o = tid + 256 * i;
        int k16 = o / 448, rem = o - k16 * 448;
        int tl = rem >> 6, lane = rem & 63, fr = lane & 15, gu = (lane >> 4) & 1, h = lane >> 5;
        const u16* sp = tile + (gu * 64 + 16 * k16 + 8 * h) * 120 + 16 * tl + fr;
        u32x4 o4;
#pragma unroll
        for (int j = 0; j < 4; ++j) o4[j] = (unsigned)sp[(2 * j) * 120] | ((unsigned)sp[(2 * j + 1) * 120] << 16);
        gstore(dst + o * 8, o4);
    }
    __syncthreads();
    k0_w2_put(wv, smem);
    __syncthreads();
    if (b < 112) k0_w2_store(b, W2, smem);
}

__device__ __forceinline__ void k0_ln(int b, const u16* __restrict__ x, u16* __restrict__ xnF, u16* __restrict__ xnT,
                                      f32x2* __restrict__ stat, char* smem) {
    const int tid = threadIdx.x, w = tid >> 6, lane = tid & 63, rr = lane >> 4, c = lane & 15;
    constexpr int TS = 516;
    u16* tile = (u16*)smem;
    const int l0 = b * 16, rl = 4 * w + rr, row = l0 + rl;
    u32x4 raw[4];
#pragma unroll
    for (int i = 0; i < 4; ++i) raw[i] = *(const u32x4*)(x + (size_t)row * D_ + (c + 16 * i) * 8);
    float v[32];
#pragma unroll
    for (int i = 0; i < 4; ++i)
#pragma unroll
        for (int j = 0; j < 4; ++j) { v[8 * i + 2 * j] = bflo(raw[i][j]); v[8 * i + 2 * j + 1] = bfhi(raw[i][j]); }
    float s = 0.f;
#pragma unroll
    for (int j = 0; j < 32; ++j) s += v[j];
    const float mean = row16_sum(s) * (1.0f / D_);
    float q2 = 0.f;
#pragma unroll
    for (int j = 0; j < 32; ++j) { float dlt = v[j] - mean; q2 += dlt * dlt; }
    const f32x2 st = ln_stats(mean, row16_sum(q2) * (1.0f / D_));
    if (c == 0) gstore(stat + row, st);
#pragma unroll
    for (int i = 0; i < 4; ++i) {
        u32x4 pb, ph;
#pragma unroll
        for (int j = 0; j < 4; ++j) {
            pb[j] = pack_bf(rbf(v[8 * i + 2 * j] - st[0]) * st[1], rbf(v[8 * i + 2 * j + 1] - st[0]) * st[1]);
            ph[j] = pack_h(bflo(pb[j]), bfhi(pb[j]));
        }
        const int ch = c + 16 * i;
        gstore(xnF + ((size_t)((row >> 5) * 32 + (ch >> 1)) * 64 + (ch & 1) * 32 + (row & 31)) * 8, pb);
        u16* tp = tile + rl * TS + ch * 8;
        *(u32x2*)(tp) = u32x2{ph[0], ph[1]};
        *(u32x2*)(tp + 4) = u32x2{ph[2], ph[3]};
    }
    __syncthreads();
#pragma unroll
    for (int it = 0; it < 4; ++it) {
        int d = it * 128 + (tid >> 1), part = tid & 1;
        const u16* sp = tile + (8 * part) * TS + d;
        u32x4 o;
#pragma unroll
        for (int j = 0; j < 4; ++j) o[j] = (unsigned)sp[(2 * j) * TS] | ((unsigned)sp[(2 * j + 1) * TS] << 16);
        gstore(xnT + (size_t)d * L_ + l0 + 8 * part, o);
    }
}

__device__ __forceinline__ int bset_idx(int e) {
    if (e < 16) return e;
    int s = (31 - __builtin_clz(e)) - 3;
    return 8 + 8 * s + ((e - (8 << s)) >> s);
}
struct cplx { float r, i; };
__device__ __forceinline__ cplx cmul(cplx a, cplx b) { return cplx{a.r * b.r - a.i * b.i, a.r * b.i + a.i * b.r}; }
__device__ __forceinline__ cplx cexp_p(float zr2, float zi2, float p) {
    float mag = __builtin_amdgcn_exp2f(zr2 * p);
    float ph = __builtin_amdgcn_fractf(zi2 * p);
    return cplx{mag * __builtin_amdgcn_cosf(ph), mag * __builtin_amdgcn_sinf(ph)};
}

constexpr int CV_RS = 528;
__device__ __forceinline__ void k0_kgen(const int d, const u16* __restrict__ lar, const u16* __restrict__ aim,
                                        const u16* __restrict__ cre, const u16* __restrict__ cim,
                                        const u16* __restrict__ ldt, char* __restrict__ ws, char* smem) {
    const int tid = threadIdx.x, lane = tid & 63;
    const int w = __builtin_amdgcn_readfirstlane(tid >> 6);
    char* Ut = smem;
    char* Vt = smem + 41 * CV_RS;
    const int n = (w & 1) * 64 + lane;
    const u16 r_ldt = ldt[d];
    const u16 r_c0 = cre[d * NS_ + lane], r_c1 = cim[d * NS_ + lane], r_c2 = cre[d * NS_ + 64 + lane], r_c3 = cim[d * NS_ + 64 + lane];
    const u16 r_lar = lar[d * NS_ + n], r_aim = aim[d * NS_ + n];
    SCHED_FENCE();
    const float dt = rbf(__expf(bf2f(r_ldt)));
    float sabs;
    {
        const float rs = row16_sum(fabsf(bf2f(r_c0)) + fabsf(bf2f(r_c1)) + fabsf(bf2f(r_c2)) + fabsf(bf2f(r_c3)));
        const int ri = __builtin_bit_cast(int, rs);
        sabs = (__builtin_bit_cast(float, __builtin_amdgcn_readlane(ri, 0)) + __builtin_bit_cast(float, __builtin_amdgcn_readlane(ri, 16)) +
                __builtin_bit_cast(float, __builtin_amdgcn_readlane(ri, 32)) + __builtin_bit_cast(float, __builtin_amdgcn_readlane(ri, 48))) * dt;
    }
    int ex = (int)((__float_as_uint(sabs) >> 23) & 0xff);
    int se = 127 + 14 - (ex - 126);
    se = se < 1 ? 1 : (se > 254 ? 254 : se);
    const float sc = __uint_as_float((unsigned)se << 23);
    {
        const float ar = -rbf(__expf(bf2f(r_lar)));
        const float zr = ar * dt, zi = bf2f(r_aim) * dt;
        const bool isU = w >= 2;
        const float pb = isU ? 16.0f : 1.0f;
        const float zr2 = zr * 1.4426950408889634f;
        const float zi2 = zi * (isU ? -0.15915494309189535f : 0.15915494309189535f);
        cplx e[8];
#pragma unroll
        for (int k = 0; k < 8; ++k) e[k] = cexp_p(zr2, zi2, pb * (float)(1 << k));
        cplx v[41];
        {
            const float crv = bf2f((w & 1) ? r_c2 : r_c0), civ = bf2f((w & 1) ? r_c3 : r_c1);
            v[0] = isU ? cplx{crv * dt * sc, -(civ * dt * sc)} : cplx{1.0f, 0.0f};
        }
        char* tb = (isU ? Ut : Vt) + n * 4;
        auto put = [&](int i0, int i1) {
#pragma unroll
            for (int i = i0; i < i1; ++i) *(unsigned*)(tb + i * CV_RS) = pack_h(v[i].r, v[i].i);
            SCHED_FENCE();
        };
        v[1] = cmul(v[0], e[0]);
#pragma unroll
        for (int k = 0; k < 2; ++k) v[2 + k] = cmul(v[k], e[1]);
#pragma unroll
        for (int k = 0; k < 4; ++k) v[4 + k] = cmul(v[k], e[2]);
#pragma unroll
        for (int k = 0; k < 8; ++k) v[8 + k] = cmul(v[k], e[3]);
        v[40] = cmul(v[0], e[7]);
        if (isU) *(unsigned*)(tb + 40 * CV_RS) = pack_h(v[40].r, v[40].i);
        put(0, 16);
#pragma unroll
        for (int k = 0; k < 8; ++k) v[16 + k] = cmul(v[2 * k], e[4]);
        put(16, 24);
#pragma unroll
        for (int k = 0; k < 8; ++k) v[24 + k] = cmul(v[k < 4 ? 4 * k : 16 + (4 * k - 16) / 2], e[5]);
        put(24, 32);
#pragma unroll
        for (int k = 0; k < 8; ++k) {
            const int ee = 8 * k;
            const int src = ee < 16 ? ee : (ee < 32 ? 16 + (ee - 16) / 2 : 24 + (ee - 32) / 4);
            v[32 + k] = cmul(v[src], e[6]);
        }
        put(32, 40);
    }
    __syncthreads();
    const int j = lane & 15, q = lane >> 4;
    {
        const int urow = (w == 0) ? j : (8 + 8 * w + j);
        const int vrow = bset_idx(j << w);
        const char* ua = Ut + urow * CV_RS + q * 16;
        const char* vb = Vt + vrow * CV_RS + q * 16;
        f16x8 a[8], bb[8];
#pragma unroll
        for (int ks = 0; ks < 8; ++ks) { a[ks] = *(const f16x8*)(ua + ks * 64); bb[ks] = *(const f16x8*)(vb + ks * 64); }
        SCHED_FENCE();
        f32x4 acc0 = {0.f, 0.f, 0.f, 0.f}, acc1 = {0.f, 0.f, 0.f, 0.f};
#pragma unroll
        for (int ks = 0; ks < 8; ks += 2) {
            acc0 = __builtin_amdgcn_mfma_f32_16x16x32_f16(a[ks], bb[ks], acc0, 0, 0, 0);
            acc1 = __builtin_amdgcn_mfma_f32_16x16x32_f16(a[ks + 1], bb[ks + 1], acc1, 0, 0, 0);
        }
        u32x2 o = {pack_h(acc0[0] + acc1[0], acc0[1] + acc1[1]), pack_h(acc0[2] + acc1[2], acc0[3] + acc1[3])};
        gstore((u16*)(ws + WS_KD) + (size_t)d * 1024 + w * 256 + j * 16 + 4 * q, o);
        if (tid == 0) ((float*)(ws + WS_KSC))[d] = 2.0f / sc;
    }
}

__global__ __launch_bounds__(256) void k0_main(const u16* __restrict__ x, const u16* __restrict__ wg,
                                               const u16* __restrict__ wu, const u16* __restrict__ wd,
                                               const u16* __restrict__ lar, const u16* __restrict__ aim,
                                               const u16* __restrict__ cre, const u16* __restrict__ cim,
                                               const u16* __restrict__ ldt, char* __restrict__ ws) {
    __shared__ __attribute__((aligned(16))) char smem[K0_SMEM];
    const unsigned pf = code_prefetch<K0_CODE>(threadIdx.x);
    int b = blockIdx.x;
    STAMP(ws, 0, 0);
    if (b < K0_KG) {
        k0_kgen(b, lar, aim, cre, cim, ldt, ws, smem);
    } else if (b < K0_KG + K0_LN) {
        __builtin_amdgcn_s_setprio(K0_PRIO);
        k0_ln(b - K0_KG, x, (u16*)(ws + WS_XNF), (u16*)(ws + WS_XNT), (f32x2*)(ws + WS_STAT), smem);
    } else {
        __builtin_amdgcn_s_setprio(K0_PRIO);
        b -= K0_KG + K0_LN;
        if (b == 0) {
#pragma unroll
            for (int i = 0; i < 48; ++i) ((unsigned*)(ws + WS_FLAG))[threadIdx.x + 256 * i] = 0u;
        }
        k0_w1(b, wg, wu, (u16*)(ws + WS_W1), wd, (u16*)(ws + WS_W2), smem);
    }
    CODE_PF_DONE(pf);
    STAMP(ws, 0, 3);
}

#define CV_SLEEP 8
#define G_PRIO 2
#define CV_TRIG 1u
constexpr int KM_CV = 256, KM_G = 256;
constexpr int G1_STAGE = 22 * 1024;
constexpr int CV_COPY = 4160;
constexpr int CV_XOFF = 8 * CV_COPY;
constexpr int CV_KD = CV_XOFF + 4608;
constexpr int CV_CH = 39968;
constexpr int KM_SMEM = 2 * CV_CH;

__device__ __forceinline__ float gelu_tanh(float x) {
    const float t = x * (-2.302208198f - 0.1029432397f * x * x);
    return x * __builtin_amdgcn_rcpf(1.0f + __builtin_amdgcn_exp2f(t));
}
__device__ __forceinline__ void glds16(unsigned ldsaddr, unsigned voff, const void* sbase) {
    asm volatile("s_mov_b32 m0, %0\n\ts_nop 0\n\tglobal_load_lds_dwordx4 %1, %2"
                 :: "s"(ldsaddr), "v"(voff), "s"(sbase) : "memory");
}
#define GLDS_OFF(voff, sbase, off) asm volatile("global_load_lds_dwordx4 %0, %1 offset:%2" :: "v"(voff), "s"(sbase), "i"(off) : "memory")
#define DS_RD128(dst, addr, off) asm volatile("ds_read_b128 %0, %1 offset:%2" : "=v"(dst) : "v"(addr), "i"(off))
typedef __attribute__((ext_vector_type(16))) float f32x16;

template <int NT>
__device__ __forceinline__ void km_gemm1_wave(const int w, const int lane, const int mb, const int nb, char* smem, char* ws) {
    const int lg = w & 3;
    constexpr int tl0 = NT == 4 ? 0 : 4;
    const unsigned lds0 = (unsigned)(uintptr_t)(LAS char*)smem;
    const int wfr = NT == 4 ? 3 * lg : (lg == 0 ? 12 : 13);
    const char* wbase = ws + WS_W1 + ((size_t)nb * 16 * 14 + wfr) * 1024;
    const char* xbase = ws + WS_XNF + ((size_t)(mb * 4 + lg) * 16) * 2048;
    const unsigned l16 = lane * 16;
    const unsigned wdst = lds0 + wfr * 1024, xdst = lds0 + (14 + 2 * lg) * 1024;
    auto stage = [&](int ks, unsigned boff) {
        const char* wb = wbase + ks * 14336;
        glds16(wdst + boff, l16, wb);
        if constexpr (NT == 4) {
            GLDS_OFF(l16, wb, 1024);
            GLDS_OFF(l16, wb, 2048);
        } else {
            const char* xb = xbase + ks * 2048;
            glds16(xdst + boff, l16, xb);
            GLDS_OFF(l16, xb, 1024);
        }
    };
    const unsigned aw = lds0 + tl0 * 1024 + l16, ax = lds0 + (14 + 2 * lg) * 1024 + l16;
    f32x16 acc[NT];
#pragma unroll
    for (int t = 0; t < NT; ++t)
#pragma unroll
        for (int i = 0; i < 16; ++i) acc[t][i] = 0.f;
    u32x4 wf[2][NT], xf[2];
    stage(0, 0);
    stage(1, G1_STAGE);
    stage(2, 2 * G1_STAGE);
    asm volatile("s_waitcnt vmcnt(6)" ::: "memory");
    __builtin_amdgcn_s_barrier();
    SCHED_FENCE();
    STAMP(ws, 1, 1);
    DS_RD128(xf[0], ax, 0);
#pragma unroll
    for (int t = 0; t < NT; ++t) DS_RD128(wf[0][t], aw, t * 1024);
    DS_RD128(xf[1], ax, 1024);
#pragma unroll
    for (int t = 0; t < NT; ++t) DS_RD128(wf[1][t], aw, 7168 + t * 1024);
    SCHED_FENCE();
    auto iter = [&](int ks, auto bufc, auto modec) {
        constexpr int BUF = decltype(bufc)::value;
        constexpr int MODE = decltype(modec)::value;
        constexpr int NB = ((BUF + 1) % 3) * G1_STAGE;
#pragma unroll
        for (int t = 0; t < NT; ++t) {
            if (t == 0) asm volatile("s_waitcnt lgkmcnt(%3)" : "+v"(wf[0][0]), "+v"(wf[0][1]), "+v"(xf[0]) : "i"(2 * NT - 1));
            if (t == 2) {
                if constexpr (NT == 4) asm volatile("s_waitcnt lgkmcnt(%2)" : "+v"(wf[0][2]), "+v"(wf[0][NT - 1]) : "i"(NT + 1));
                else asm volatile("s_waitcnt lgkmcnt(%1)" : "+v"(wf[0][2]) : "i"(NT + 1));
            }
            acc[t] = __builtin_amdgcn_mfma_f32_32x32x16_bf16(__builtin_bit_cast(bf16x8, wf[0][t]), __builtin_bit_cast(bf16x8, xf[0]), acc[t], 0, 0, 0);
            SCHED_FENCE();
        }
        if constexpr (NT == 4) asm volatile("s_waitcnt lgkmcnt(0)" : "+v"(xf[1]), "+v"(wf[1][0]), "+v"(wf[1][1]), "+v"(wf[1][2]), "+v"(wf[1][NT - 1]) :: "memory");
        else asm volatile("s_waitcnt lgkmcnt(0)" : "+v"(xf[1]), "+v"(wf[1][0]), "+v"(wf[1][1]), "+v"(wf[1][2]) :: "memory");
        if (MODE < 3) {
            if (MODE < 2) asm volatile("s_waitcnt vmcnt(3)" ::: "memory");
            else asm volatile("s_waitcnt vmcnt(0)" ::: "memory");
            SCHED_FENCE();
            __builtin_amdgcn_s_barrier();
        }
        SCHED_FENCE();
#pragma unroll
        for (int t = 0; t < NT; ++t) {
            acc[t] = __builtin_amdgcn_mfma_f32_32x32x16_bf16(__builtin_bit_cast(bf16x8, wf[1][t]), __builtin_bit_cast(bf16x8, xf[1]), acc[t], 0, 0, 0);
            SCHED_FENCE();
            if (MODE < 3 && t == 0) {
                DS_RD128(xf[0], ax, NB);
#pragma unroll
                for (int u = 0; u < NT; ++u) DS_RD128(wf[0][u], aw, NB + u * 1024);
            }
            if (MODE == 0 && t == 1) stage(ks + 3, BUF * G1_STAGE);
            SCHED_FENCE();
        }
        if (MODE < 3) {
            DS_RD128(xf[1], ax, NB + 1024);
#pragma unroll
            for (int t = 0; t < NT; ++t) DS_RD128(wf[1][t], aw, NB + 7168 + t * 1024);
        }
        SCHED_FENCE();
    };
    using I0 = std::integral_constant<int, 0>; using I1 = std::integral_constant<int, 1>;
    using I2 = std::integral_constant<int, 2>; using I3 = std::integral_constant<int, 3>;
#pragma unroll 1
    for (int k3 = 0; k3 < 12; k3 += 3) {
        iter(k3, I0{}, I0{});
        iter(k3 + 1, I1{}, I0{});
        iter(k3 + 2, I2{}, I0{});
    }
    iter(12, I0{}, I0{});
    iter(13, I1{}, I1{});
    iter(14, I2{}, I2{});
    iter(15, I0{}, I3{});
    __builtin_amdgcn_s_barrier();
    if (NT == 4 && w == 0 && lane < 4)
        __hip_atomic_fetch_add((GAS unsigned*)(ws + WS_FLAG) + 64 * (64 + 8 * mb + 4 + lane), 1u, __ATOMIC_RELAXED, __HIP_MEMORY_SCOPE_AGENT);
    STAMP(ws, 1, 2);
    const int hh = lane >> 5;
    const int lt = 8 * mb + 2 * lg + ((lane >> 4) & 1);
#pragma unroll
    for (int t = 0; t < NT; ++t) {
        const int T = 7 * nb + tl0 + t;
        u32x4 p;
#pragma unroll
        for (int i2 = 0; i2 < 4; ++i2) {
            float hv[2];
#pragma unroll
            for (int r = 0; r < 2; ++r) {
                const float g = rbf(acc[t][2 * i2 + r]), u = rbf(acc[t][8 + 2 * i2 + r]);
                hv[r] = rbf(gelu_tanh(g)) * u;
            }
            p[i2] = pack_bf(hv[0], hv[1]);
        }
        gstore(ws + WS_HF + ((size_t)(lt * 56 + (T >> 1)) * 64 + (2 * (T & 1) + hh) * 16 + (lane & 15)) * 16, p);
    }
}

struct IdxTab { u16 v[2064]; };
constexpr IdxTab make_idx_tab() {
    IdxTab t{};
    for (int i = 0; i < 2064; ++i) {
        int lag = 2047 - i;
        if (lag < 0) { t.v[i] = 1024; continue; }
        int p = lag;
        if (lag >= 256) {
            int s = 0;
            while ((lag >> s) >= 256) ++s;
            int step = 1 << s, rem = lag & (step - 1), base = lag - rem, half = step >> 1;
            if (rem > half || (rem == half && ((base >> s) & 1))) base += step;
            p = base;
        }
        int e = 0, r = p;
        if (p >= 256) {
            while ((p >> e) >= 256) ++e;
            if (e > 3) e = 3;
            r = (p - (128 << e)) >> e;
        }
        t.v[i] = (u16)(e * 256 + (r & 15) * 16 + (r >> 4));
    }
    return t;
}
__device__ const IdxTab kIdxTab = make_idx_tab();

__device__ __forceinline__ void km_conv(const int cb, char* __restrict__ ws, char* smem0) {
    const int tid = threadIdx.x & 255, lane = tid & 63;
    const int w8 = __builtin_amdgcn_readfirstlane(threadIdx.x >> 6);
    const int ch = w8 >> 2, w = w8 & 3;
    const int d = 2 * cb + ch;
    char* smem = smem0 + ch * CV_CH;
    u16* Kd = (u16*)(smem + CV_KD);
    const u32x2 kdv = *(const u32x2*)((const u16*)(ws + WS_KD) + (size_t)d * 1024 + tid * 4);
    const float osc = ((const float*)(ws + WS_KSC))[d];
    const u32x4 it0 = *(const u32x4*)(kIdxTab.v + 8 * tid), it1 = *(const u32x4*)(kIdxTab.v + 8 * tid + 8);
    const u32x4 xrow = *(const u32x4*)((const u16*)(ws + WS_XNT) + (size_t)d * L_ + tid * 8);
    SCHED_FENCE();
    *(u32x2*)(Kd + tid * 4) = kdv;
    if (tid == 0) Kd[1024] = 0;
    if (w8 == 0) {
        GAS unsigned* cnt = (GAS unsigned*)(ws + WS_FLAG) + 64 * (64 + 8 * (cb & 15) + 4 + ((cb >> 4) & 3));
        for (unsigned spins = 0; spins < 128; ++spins) {
            if (__builtin_amdgcn_readfirstlane(__hip_atomic_load(cnt, __ATOMIC_RELAXED, __HIP_MEMORY_SCOPE_AGENT)) >= CV_TRIG) break;
            __builtin_amdgcn_s_sleep(CV_SLEEP);
        }
    }
    __syncthreads();
    STAMP(ws, 1, 1);
    const int j = lane & 15, q = lane >> 4;
    {
        unsigned wd[8];
#pragma unroll
        for (int e = 0; e < 4; ++e) {
            wd[e] = (unsigned)Kd[it0[e] & 0xffff] | ((unsigned)Kd[it0[e] >> 16] << 16);
            wd[4 + e] = (unsigned)Kd[it1[e] & 0xffff] | ((unsigned)Kd[it1[e] >> 16] << 16);
        }
        char* dst = smem + tid * 16;
#pragma unroll
        for (int e = 0; e < 4; ++e) {
            u32x4 ev = {wd[e], wd[e + 1], wd[e + 2], wd[e + 3]};
            *(u32x4*)(dst + (2 * e) * CV_COPY) = ev;
            u32x4 od;
#pragma unroll
            for (int k = 0; k < 4; ++k) od[k] = __builtin_amdgcn_alignbit(wd[e + k + 1], wd[e + k], 16);
            *(u32x4*)(dst + (2 * e + 1) * CV_COPY) = od;
        }
        char* xp = smem + CV_XOFF;
        if (tid < 32) *(u32x4*)(xp + tid * 16) = u32x4{0u, 0u, 0u, 0u};
        if (tid >= 64 && tid < 72) *(u32x4*)(smem + (tid - 64) * CV_COPY + 256 * 16) = u32x4{0u, 0u, 0u, 0u};
        *(u32x4*)(xp + 512 + tid * 16) = xrow;
    }
    __syncthreads();
    STAMP(ws, 1, 2);
    const char* kb = smem + ((7 - j) & 7) * CV_COPY + (253 + q - (j >> 3)) * 16 - w * 2 * 64;
    const char* xq = smem + CV_XOFF + (256 + 16 * j - 16 + 8 * q) * 2 - w * 2 * 64;
    f32x4 acc[8];
#pragma unroll
    for (int i = 0; i < 8; ++i) acc[i] = f32x4{0.f, 0.f, 0.f, 0.f};
    {
        f16x8 bfr[2][8], af[3];
#pragma unroll
        for (int c = 0; c < 8; ++c) bfr[0][c] = *(const f16x8*)(xq + c * 512);
        af[0] = *(const f16x8*)(kb);
        af[1] = *(const f16x8*)(kb - 8 * 64);
        SCHED_FENCE();
#pragma unroll
        for (int ri = 0; ri < 2; ++ri) {
#pragma unroll
            for (int mm = 0; mm < 8; ++mm) {
                const int g = ri * 8 + mm;
                if (g + 2 < 16) {
                    const int g2 = g + 2;
                    af[g2 % 3] = *(const f16x8*)(kb - (8 * (g2 & 7) + (g2 >> 3)) * 64);
                }
                if (ri < 1) bfr[1][mm] = *(const f16x8*)(xq + mm * 512 - 64);
#pragma unroll
                for (int ct = mm; ct < 8; ++ct)
                    acc[ct] = __builtin_amdgcn_mfma_f32_16x16x32_f16(af[g % 3], bfr[ri][ct - mm], acc[ct], 0, 0, 0);
                SCHED_FENCE();
            }
        }
    }
    STAMP(ws, 1, 4);
    __syncthreads();
    {
        f32x4* exb = (f32x4*)(smem + w * 8192) + lane;
#pragma unroll
        for (int i = 0; i < 8; ++i) exb[i * 64] = acc[i];
    }
    __syncthreads();
    {
        u16* yp = (u16*)(ws + WS_YT) + (size_t)d * L_ + 16 * j + 4 * q + 512 * w;
#pragma unroll
        for (int i = 0; i < 2; ++i) {
            const f32x4* exb = (const f32x4*)(smem + (2 * w + i) * 1024) + lane;
            f32x4 o = exb[0] + exb[512] + exb[1024] + exb[1536];
            u32x2 p = {pack_bf(o[0] * osc, o[1] * osc), pack_bf(o[2] * osc, o[3] * osc)};
            gstore(yp + 256 * i, p);
        }
    }
    STAMP(ws, 1, 6);
    asm volatile("s_waitcnt vmcnt(0)" ::: "memory");
    __syncthreads();
    if (threadIdx.x < 8) __hip_atomic_fetch_add((GAS unsigned*)(ws + WS_FLAG) + 64 * (8 * (cb >> 5) + threadIdx.x), 1u, __ATOMIC_RELAXED, __HIP_MEMORY_SCOPE_AGENT);
}

struct G2Map { int lb, db; };
__device__ __forceinline__ G2Map g2_map(int bid) { const int xcd = bid & 7, i5 = bid >> 3; return G2Map{4 * xcd + (i5 & 3), i5 >> 2}; }
__device__ __forceinline__ void g2_stage_w(const int bid, const int w8, const int lane, const int k, char* ws, char* smem) {
    const int kg = w8 & 3, lh = w8 >> 2;
    const unsigned lds0 = (unsigned)(uintptr_t)(LAS char*)smem;
    const char* src = ws + WS_W2 + ((size_t)(g2_map(bid).db * 56 + 14 * kg + k) * 4 + 2 * lh) * 1024;
    const unsigned l16 = lane * 16;
    glds16(lds0 + (k & 3) * 16384 + kg * 4096 + lh * 2048, l16, src);
    GLDS_OFF(l16, src, 1024);
}
#define HLOAD(dst, voff, base, off) asm volatile("global_load_dwordx4 %0, %1, %2 offset:%3 sc1" : "=v"(dst) : "v"(voff), "s"(base), "i"(off))
__device__ __forceinline__ void km_gemm2(const int bid, const int w8, const int lane, const u16* __restrict__ x,
                                         const u16* __restrict__ dskip, char* __restrict__ ws, u16* __restrict__ out, char* smem) {
    const int kg = w8 & 3, lh = w8 >> 2;
    const int xcd = bid & 7;
    const int lb = g2_map(bid).lb, db = g2_map(bid).db;
    const int r = lane & 15, q = lane >> 4;
    const __amdgpu_buffer_rsrc_t yrs = __builtin_amdgcn_make_buffer_rsrc(ws + WS_YT, 0, D_ * L_ * 2, 0x00020000);
    const int l16 = lane * 16;
    const char* hb0 = ws + WS_HF + (size_t)((4 * lb + 2 * lh) * 56 + 14 * kg) * 1024;
    const char* hb1 = hb0 + 56 * 1024;
    const unsigned awr = (unsigned)(uintptr_t)(LAS char*)smem + kg * 4096 + l16;
    if (w8 == 0) {
        GAS unsigned* cnt = (GAS unsigned*)(ws + WS_FLAG) + 64 * (64 + 8 * (lb >> 1) + (db & 3));
        for (unsigned spins = 0; spins < (1u << 22); ++spins) {
            if (__builtin_amdgcn_readfirstlane(__hip_atomic_load(cnt, __ATOMIC_RELAXED, __HIP_MEMORY_SCOPE_AGENT)) == 16u) break;
            __builtin_amdgcn_s_sleep(4);
        }
    }
    __syncthreads();
    STAMP(ws, 1, 5);
    u32x4 hf[3][2], wf[4];
#pragma unroll
    for (int s = 0; s < 3; ++s) { HLOAD(hf[s][0], l16, hb0, s * 1024); HLOAD(hf[s][1], l16, hb1, s * 1024); }
    SCHED_FENCE();
    f32x4 acc[4][2];
#pragma unroll
    for (int t = 0; t < 4; ++t)
#pragma unroll
        for (int i = 0; i < 2; ++i) acc[t][i] = f32x4{0.f, 0.f, 0.f, 0.f};
    const int oi = kg >> 1, ot = 2 * (kg & 1);
    const int lrow = 64 * lb + 32 * lh + 16 * oi + r;
    const int dcol = 64 * db + 16 * q + 4 * ot;
    auto poll = [&]() {
        return __hip_atomic_load((GAS unsigned*)(ws + WS_FLAG) + 64 * (8 * db + xcd), __ATOMIC_RELAXED, __HIP_MEMORY_SCOPE_AGENT);
    };
    auto ldy = [&]() {
        int ln = l16; asm volatile("" : "+v"(ln));
        const int row = ln >> 5;
        const int yo = (16 * (row >> 3) + (row & 7)) * (L_ * 2) + (ln & 16);
        const int so = ((64 * db + 4 * ot) * L_ + 64 * lb + 32 * lh + 16 * oi) * 2;
        return __builtin_amdgcn_raw_buffer_load_b128(yrs, yo, so, 16);
    };
    unsigned fv = 0;
    bool early = false;
    u32x4 yv, xv, dv;
    f32x2 st;
#pragma unroll
    for (int k = 0; k < 14; ++k) {
        const int s = k % 3;
        constexpr int kN[14] = {4, 6, 8, 8, 8, 8, 8, 8, 8, 8, 8, 8, 4, 0};
        asm volatile("s_waitcnt vmcnt(%2)" : "+v"(hf[s][0]), "+v"(hf[s][1]) : "i"(kN[k]) : "memory");
        __builtin_amdgcn_s_barrier();
        SCHED_FENCE();
#pragma unroll
        for (int t = 0; t < 4; ++t) DS_RD128(wf[t], awr, (k & 3) * 16384 + t * 1024);
#pragma unroll
        for (int t = 0; t < 4; ++t) {
            asm volatile("s_waitcnt lgkmcnt(%1)" : "+v"(wf[t]) : "i"(3 - t));
#pragma unroll
            for (int i = 0; i < 2; ++i)
                acc[t][i] = __builtin_amdgcn_mfma_f32_16x16x32_bf16(__builtin_bit_cast(bf16x8, wf[t]), __builtin_bit_cast(bf16x8, hf[s][i]), acc[t][i], 0, 0, 0);
            SCHED_FENCE();
        }
        if (k + 3 < 14) {
            g2_stage_w(bid, w8, lane, k + 3, ws, smem);
            const char* h0 = hb0 + (k + 3) * 1024;
            const char* h1 = hb1 + (k + 3) * 1024;
            HLOAD(hf[s][0], l16, h0, 0);
            HLOAD(hf[s][1], l16, h1, 0);
        }
        if (k == 11) fv = poll();
        if (k == 13) {
            early = __builtin_amdgcn_readfirstlane(fv) == 32u;
            if (early) yv = ldy();
            xv = *(const u32x4*)(x + (size_t)lrow * D_ + dcol);
            dv = *(const u32x4*)(dskip + dcol);
            st = ((const f32x2*)(ws + WS_STAT))[lrow];
        }
        SCHED_FENCE();
    }
    __builtin_amdgcn_s_barrier();
    STAMP(ws, 1, 6);
    {
        f32x4* red = (f32x4*)smem + lh * (24 * 64);
#pragma unroll
        for (int dk = 0; dk < 4; ++dk) {
            if (dk != kg) {
                const int si = kg < dk ? kg : kg - 1;
#pragma unroll
                for (int tt = 0; tt < 2; ++tt) red[((dk * 3 + si) * 2 + tt) * 64 + lane] = acc[2 * (dk & 1) + tt][dk >> 1];
            }
        }
    }
    if (w8 == 0 && !early) {
        for (unsigned spins = 0; spins < (1u << 22); ++spins) {
            if (__builtin_amdgcn_readfirstlane(poll()) == 32u) break;
            __builtin_amdgcn_s_sleep(8);
        }
    }
    __syncthreads();
    if (!early) yv = ldy();
    STAMP(ws, 1, 7);
    f32x4 f[2];
    {
        const f32x4* red = (const f32x4*)smem + lh * (24 * 64);
#pragma unroll
        for (int tt = 0; tt < 2; ++tt)
            f[tt] = kg == 0 ? acc[tt][0] : (kg == 1 ? acc[2 + tt][0] : (kg == 2 ? acc[tt][1] : acc[2 + tt][1]));
#pragma unroll
        for (int si = 0; si < 3; ++si)
#pragma unroll
            for (int tt = 0; tt < 2; ++tt) f[tt] += red[((kg * 3 + si) * 2 + tt) * 64 + lane];
    }
    u16 yt[8];
    {
        char* yb = smem + lh * 24576 + kg * 6144;
        asm volatile("" ::: "memory");
        *(u32x4*)(yb + lane * 16) = yv;
        asm volatile("s_waitcnt lgkmcnt(0)" ::: "memory");
        const u16* yr = (const u16*)(yb + q * 256 + 2 * r);
#pragma unroll
        for (int e = 0; e < 8; ++e) yt[e] = yr[e * 16];
    }
    u32x4 ov;
#pragma unroll
    for (int jj = 0; jj < 4; ++jj) {
        float res[2];
#pragma unroll
        for (int hh = 0; hh < 2; ++hh) {
            const int e = 2 * jj + hh;
            const float fv2 = rbf(f[e >> 2][e & 3]);
            const float g = rbf(fv2 * __builtin_amdgcn_rcpf(1.0f + __expf(-fv2)));
            const float ds = hh ? bfhi(dv[jj]) : bflo(dv[jj]);
            const float xx = hh ? bfhi(xv[jj]) : bflo(xv[jj]);
            const float xn = rbf(rbf(xx - st[0]) * st[1]);
            const float s = rbf(bf2f(yt[e]) + rbf(xn * ds));
            res[hh] = xx + rbf(s * g);
        }
        ov[jj] = pack_bf(res[0], res[1]);
    }
    *(u32x4*)(out + (size_t)lrow * D_ + dcol) = ov;
}

__global__ __launch_bounds__(512, 4) void km_main(const u16* __restrict__ x, const u16* __restrict__ dskip,
                                                  char* __restrict__ ws, u16* __restrict__ out) {
    __shared__ __attribute__((aligned(16))) char smem[KM_SMEM];
    const int b = blockIdx.x;
    const unsigned pf = code_prefetch<KM_CODE>(threadIdx.x, b >= KM_G);
    STAMP(ws, 1, 0);
    if (b < KM_G) {
        const int lane = threadIdx.x & 63;
        const int w = __builtin_amdgcn_readfirstlane(threadIdx.x >> 6);
        const int xcd = b & 7, i5 = b >> 3;
        const int mb = (xcd & 1) * 8 + (i5 & 7), nb = (xcd >> 1) * 4 + (i5 >> 3);
        __builtin_amdgcn_s_setprio(G_PRIO);
        if (w < 4) km_gemm1_wave<4>(w, lane, mb, nb, smem, ws);
        else km_gemm1_wave<3>(w, lane, mb, nb, smem, ws);
#pragma unroll
        for (int k = 0; k < 3; ++k) g2_stage_w(b, w, lane, k, ws, smem);
        asm volatile("s_waitcnt vmcnt(6)" ::: "memory");
        __builtin_amdgcn_s_barrier();
        if (threadIdx.x < 4) __hip_atomic_fetch_add((GAS unsigned*)(ws + WS_FLAG) + 64 * (64 + 8 * mb + threadIdx.x), 1u, __ATOMIC_RELAXED, __HIP_MEMORY_SCOPE_AGENT);
        STAMP(ws, 1, 4);
        km_gemm2(b, w, lane, x, dskip, ws, out, smem);
    } else {
        km_conv(b - KM_G, ws, smem);
        CODE_PF_DONE(pf);
    }
    STAMP(ws, 1, 3);
}

extern "C" void kernel_launch(void* const* d_in, const int* in_sizes, int n_in,
                              void* d_out, int out_size, void* d_ws, size_t ws_size,
                              hipStream_t stream) {
    const u16* x   = (const u16*)d_in[0];
    const u16* lar = (const u16*)d_in[1];
    const u16* aim = (const u16*)d_in[2];
    const u16* cre = (const u16*)d_in[3];
    const u16* cim = (const u16*)d_in[4];
    const u16* ldt = (const u16*)d_in[5];
    const u16* dsk = (const u16*)d_in[6];
    const u16* wg  = (const u16*)d_in[8];
    const u16* wu  = (const u16*)d_in[9];
    const u16* wd  = (const u16*)d_in[10];
    char* ws = (char*)d_ws;
    k0_main<<<K0_GRID, 256, 0, stream>>>(x, wg, wu, wd, lar, aim, cre, cim, ldt, ws);
    km_main<<<KM_G + KM_CV, 512, 0, stream>>>(x, dsk, ws, (u16*)d_out);
}
```

```cpp
#include <hip/hip_runtime.h>
#include <hip/hip_bf16.h>
#include <stdint.h>
#include <type_traits>
#define KM_PF_ALL 1

typedef __attribute__((ext_vector_type(8))) __bf16 bf16x8;
typedef __attribute__((ext_vector_type(2))) __bf16 bf16x2;
typedef __attribute__((ext_vector_type(8))) _Float16 f16x8;
typedef __attribute__((ext_vector_type(4))) float f32x4;
typedef __attribute__((ext_vector_type(2))) float f32x2;
typedef __attribute__((ext_vector_type(4))) unsigned int u32x4;
typedef __attribute__((ext_vector_type(2))) unsigned int u32x2;
typedef unsigned short u16;
#define GAS __attribute__((address_space(1)))
#define LAS __attribute__((address_space(3)))

constexpr int L_ = 2048, D_ = 512, NS_ = 128, F_ = 1792;

constexpr size_t WS_XNT  = 0;
constexpr size_t WS_STAT = 2u << 20;
constexpr size_t WS_FLAG = (2u << 20) + 65536;
constexpr size_t WS_W2   = 3u << 20;
constexpr size_t WS_HF   = 5u << 20;
constexpr size_t WS_YT   = 13u << 20;
constexpr size_t WS_KD   = 15u << 20;
constexpr size_t WS_KSC  = 17u << 20;
constexpr size_t WS_XNF  = 18u << 20;
constexpr size_t WS_W1   = 20u << 20;

#define STAMP(ws_, kid, slot) do {} while (0)
#define SCHED_FENCE() __builtin_amdgcn_sched_barrier(0)

template <typename T> __device__ __forceinline__ void gstore(void* p, T v) {
    if constexpr (sizeof(T) == 16) asm volatile("global_store_dwordx4 %0, %1, off sc0 sc1\n\ts_nop 1" :: "v"(p), "v"(v));
    else asm volatile("global_store_dwordx2 %0, %1, off sc0 sc1\n\ts_nop 1" :: "v"(p), "v"(v));
}
__device__ __forceinline__ float bf2f(u16 v) { return __uint_as_float(((unsigned)v) << 16); }
__device__ __forceinline__ float bflo(unsigned v) { return __uint_as_float(v << 16); }
__device__ __forceinline__ float bfhi(unsigned v) { return __uint_as_float(v & 0xffff0000u); }
__device__ __forceinline__ u16 f2bf(float f) { __bf16 b = (__bf16)f; return __builtin_bit_cast(u16, b); }
__device__ __forceinline__ float rbf(float f) { return bf2f(f2bf(f)); }
__device__ __forceinline__ unsigned pack_bf(float lo, float hi) {
    return (unsigned)f2bf(lo) | ((unsigned)f2bf(hi) << 16);
}
__device__ __forceinline__ u16 f2h(float f) { _Float16 h = (_Float16)f; return __builtin_bit_cast(u16, h); }
typedef __attribute__((ext_vector_type(2))) _Float16 f16x2;
__device__ __forceinline__ unsigned pack_h(float lo, float hi) {
    return (unsigned)f2h(lo) | ((unsigned)f2h(hi) << 16);
}
__device__ __forceinline__ float wave_sum(float v) {
#pragma unroll
    for (int o = 32; o > 0; o >>= 1) v += __shfl_xor(v, o, 64);
    return v;
}
__device__ __forceinline__ float row16_sum(float v) {
    v += __builtin_bit_cast(float, __builtin_amdgcn_update_dpp(0, __builtin_bit_cast(int, v), 0x128, 0xf, 0xf, false));
    v += __builtin_bit_cast(float, __builtin_amdgcn_update_dpp(0, __builtin_bit_cast(int, v), 0x124, 0xf, 0xf, false));
    v += __builtin_bit_cast(float, __builtin_amdgcn_update_dpp(0, __builtin_bit_cast(int, v), 0x122, 0xf, 0xf, false));
    v += __builtin_bit_cast(float, __builtin_amdgcn_update_dpp(0, __builtin_bit_cast(int, v), 0x121, 0xf, 0xf, false));
    return v;
}
__device__ __forceinline__ float dot2bf(unsigned a, unsigned b, float c) {
    return __builtin_amdgcn_fdot2_f32_bf16(__builtin_bit_cast(bf16x2, a), __builtin_bit_cast(bf16x2, b), c, false);
}
template <int BYTES> __device__ __forceinline__ unsigned code_prefetch(int t, bool on = true) {
    unsigned acc = 0;
    unsigned long long pc;
    asm volatile("s_getpc_b64 %0" : "=s"(pc));
    const char* base = (const char*)(pc & ~(unsigned long long)127);
    if (on && t * 128 < BYTES) acc = *(const volatile unsigned*)(base + t * 128);
    return acc;
}
#define CODE_PF_DONE(v) asm volatile("" :: "v"(v))
__device__ __forceinline__ f32x2 ln_stats(float mean, float var) {
    const float mb = rbf(mean);
    const float sd = rbf(__builtin_sqrtf(rbf(rbf(var) + 1e-5f)));
    return f32x2{mb, 1.0f / sd};
}

#define K0_PRIO 1
constexpr int K0_W1 = 128, K0_LN = 128, K0_KG = 512;
constexpr int K0_GRID = K0_W1 + K0_LN + K0_KG;
constexpr int K0_SMEM = 44 * 1024;
constexpr int K0_CODE = 11264, KM_CODE = 17920;

__device__ __forceinline__ void k0_w2_load(int b, const u16* __restrict__ wd, u32x4 (&v)[4]) {
    const int tid = threadIdx.x, ks = b >> 1, dh = b & 1;
#pragma unroll
    for (int i = 0; i < 4; ++i) {
        int c = tid + 256 * i, r = c >> 5, ch = c & 31;
        v[i] = *(const u32x4*)(wd + (size_t)(32 * ks + r) * D_ + 256 * dh + ch * 8);
    }
}
__device__ __forceinline__ void k0_w2_put(const u32x4 (&v)[4], char* smem) {
    const int tid = threadIdx.x;
    u16* tile = (u16*)smem;
#pragma unroll
    for (int i = 0; i < 4; ++i) {
        int c = tid + 256 * i, r = c >> 5, ch = c & 31;
        *(u32x4*)(tile + r * 264 + ch * 8) = v[i];
    }
}
__device__ __forceinline__ void k0_w2_store(int b, u16* __restrict__ W2, const char* smem) {
    const int tid = threadIdx.x, ks = b >> 1, dh = b & 1;
    const u16* tile = (const u16*)smem;
#pragma unroll
    for (int dbl = 0; dbl < 4; ++dbl) {
        int t = tid >> 6, lane = tid & 63, r = lane & 15, q = lane >> 4;
        int dl = 64 * dbl + 16 * (r >> 2) + 4 * t + (r & 3);
        const u16* sp = tile + (16 * (q >> 1) + 4 * (q & 1)) * 264 + dl;
        u32x4 o4;
        o4[0] = (unsigned)sp[0 * 264] | ((unsigned)sp[1 * 264] << 16);
        o4[1] = (unsigned)sp[2 * 264] | ((unsigned)sp[3 * 264] << 16);
        o4[2] = (unsigned)sp[8 * 264] | ((unsigned)sp[9 * 264] << 16);
        o4[3] = (unsigned)sp[10 * 264] | ((unsigned)sp[11 * 264] << 16);
        gstore(W2 + ((size_t)((4 * dh + dbl) * 56 + ks) * 4 + t) * 512 + lane * 8, o4);
    }
}

__device__ __forceinline__ void k0_w1(int b, const u16* __restrict__ wg, const u16* __restrict__ wu,
                                      u16* __restrict__ W1, const u16* __restrict__ wd, u16* __restrict__ W2, char* smem) {
    const int nb = b >> 3, kp = b & 7, tid = threadIdx.x;
    u16* tile = (u16*)smem;
    u32x4 v[7];
#pragma unroll
    for (int i = 0; i < 7; ++i) {
        int c = tid + 256 * i;
        int gu = c / 896, rem = c - gu * 896, r = rem / 14, ch = rem - r * 14;
        v[i] = *(const u32x4*)((gu ? wu : wg) + (size_t)(64 * kp + r) * F_ + 112 * nb + ch * 8);
    }
    u32x4 wv[4];
    k0_w2_load(b < 112 ? b : 111, wd, wv);
    SCHED_FENCE();
#pragma unroll
    for (int i = 0; i < 7; ++i) {
        int c = tid + 256 * i;
        int gu = c / 896, rem = c - gu * 896, r = rem / 14, ch = rem - r * 14;
        *(u32x4*)(tile + (gu * 64 + r) * 120 + ch * 8) = v[i];
    }
    __syncthreads();
    u16* dst = W1 + (size_t)(nb * 16 + 2 * kp) * 14 * 512;
#pragma unroll
    for (int i = 0; i < 7; ++i) {
        int o = tid + 256 * i;
        int k16 = o / 448, rem = o - k16 * 448;
        int tl = rem >> 6, lane = rem & 63, fr = lane & 15, gu = (lane >> 4) & 1, h = lane >> 5;
        const u16* sp = tile + (gu * 64 + 16 * k16 + 8 * h) * 120 + 16 * tl + fr;
        u32x4 o4;
#pragma unroll
        for (int j = 0; j < 4; ++j) o4[j] = (unsigned)sp[(2 * j) * 120] | ((unsigned)sp[(2 * j + 1) * 120] << 16);
        gstore(dst + o * 8, o4);
    }
    __syncthreads();
    k0_w2_put(wv, smem);
    __syncthreads();
    if (b < 112) k0_w2_store(b, W2, smem);
}

__device__ __forceinline__ void k0_ln(int b, const u16* __restrict__ x, u16* __restrict__ xnF, u16* __restrict__ xnT,
                                      f32x2* __restrict__ stat, char* smem) {
    const int tid = threadIdx.x, w = tid >> 6, lane = tid & 63, rr = lane >> 4, c = lane & 15;
    constexpr int TS = 516;
    u16* tile = (u16*)smem;
    const int l0 = b * 16, rl = 4 * w + rr, row = l0 + rl;
    u32x4 raw[4];
#pragma unroll
    for (int i = 0; i < 4; ++i) raw[i] = *(const u32x4*)(x + (size_t)row * D_ + (c + 16 * i) * 8);
    float v[32];
#pragma unroll
    for (int i = 0; i < 4; ++i)
#pragma unroll
        for (int j = 0; j < 4; ++j) { v[8 * i + 2 * j] = bflo(raw[i][j]); v[8 * i + 2 * j + 1] = bfhi(raw[i][j]); }
    float s = 0.f;
#pragma unroll
    for (int j = 0; j < 32; ++j) s += v[j];
    const float mean = row16_sum(s) * (1.0f / D_);
    float q2 = 0.f;
#pragma unroll
    for (int j = 0; j < 32; ++j) { float dlt = v[j] - mean; q2 += dlt * dlt; }
    const f32x2 st = ln_stats(mean, row16_sum(q2) * (1.0f / D_));
    if (c == 0) gstore(stat + row, st);
#pragma unroll
    for (int i = 0; i < 4; ++i) {
        u32x4 pb, ph;
#pragma unroll
        for (int j = 0; j < 4; ++j) {
            pb[j] = pack_bf(rbf(v[8 * i + 2 * j] - st[0]) * st[1], rbf(v[8 * i + 2 * j + 1] - st[0]) * st[1]);
            ph[j] = pack_h(bflo(pb[j]), bfhi(pb[j]));
        }
        const int ch = c + 16 * i;
        gstore(xnF + ((size_t)((row >> 5) * 32 + (ch >> 1)) * 64 + (ch & 1) * 32 + (row & 31)) * 8, pb);
        u16* tp = tile + rl * TS + ch * 8;
        *(u32x2*)(tp) = u32x2{ph[0], ph[1]};
        *(u32x2*)(tp + 4) = u32x2{ph[2], ph[3]};
    }
    __syncthreads();
#pragma unroll
    for (int it = 0; it < 4; ++it) {
        int d = it * 128 + (tid >> 1), part = tid & 1;
        const u16* sp = tile + (8 * part) * TS + d;
        u32x4 o;
#pragma unroll
        for (int j = 0; j < 4; ++j) o[j] = (unsigned)sp[(2 * j) * TS] | ((unsigned)sp[(2 * j + 1) * TS] << 16);
        gstore(xnT + (size_t)d * L_ + l0 + 8 * part, o);
    }
}

__device__ __forceinline__ int bset_idx(int e) {
    if (e < 16) return e;
    int s = (31 - __builtin_clz(e)) - 3;
    return 8 + 8 * s + ((e - (8 << s)) >> s);
}
struct cplx { float r, i; };
__device__ __forceinline__ cplx cmul(cplx a, cplx b) { return cplx{a.r * b.r - a.i * b.i, a.r * b.i + a.i * b.r}; }
__device__ __forceinline__ cplx cexp_p(float zr2, float zi2, float p) {
    float mag = __builtin_amdgcn_exp2f(zr2 * p);
    float ph = __builtin_amdgcn_fractf(zi2 * p);
    return cplx{mag * __builtin_amdgcn_cosf(ph), mag * __builtin_amdgcn_sinf(ph)};
}

constexpr int CV_RS = 528;
__device__ __forceinline__ void k0_kgen(const int d, const u16* __restrict__ lar, const u16* __restrict__ aim,
                                        const u16* __restrict__ cre, const u16* __restrict__ cim,
                                        const u16* __restrict__ ldt, char* __restrict__ ws, char* smem) {
    const int tid = threadIdx.x, lane = tid & 63;
    const int w = __builtin_amdgcn_readfirstlane(tid >> 6);
    char* Ut = smem;
    char* Vt = smem + 41 * CV_RS;
    const int n = (w & 1) * 64 + lane;
    const u16 r_ldt = ldt[d];
    const u16 r_c0 = cre[d * NS_ + lane], r_c1 = cim[d * NS_ + lane], r_c2 = cre[d * NS_ + 64 + lane], r_c3 = cim[d * NS_ + 64 + lane];
    const u16 r_lar = lar[d * NS_ + n], r_aim = aim[d * NS_ + n];
    SCHED_FENCE();
    const float dt = rbf(__expf(bf2f(r_ldt)));
    float sabs;
    {
        const float rs = row16_sum(fabsf(bf2f(r_c0)) + fabsf(bf2f(r_c1)) + fabsf(bf2f(r_c2)) + fabsf(bf2f(r_c3)));
        const int ri = __builtin_bit_cast(int, rs);
        sabs = (__builtin_bit_cast(float, __builtin_amdgcn_readlane(ri, 0)) + __builtin_bit_cast(float, __builtin_amdgcn_readlane(ri, 16)) +
                __builtin_bit_cast(float, __builtin_amdgcn_readlane(ri, 32)) + __builtin_bit_cast(float, __builtin_amdgcn_readlane(ri, 48))) * dt;
    }
    int ex = (int)((__float_as_uint(sabs) >> 23) & 0xff);
    int se = 127 + 14 - (ex - 126);
    se = se < 1 ? 1 : (se > 254 ? 254 : se);
    const float sc = __uint_as_float((unsigned)se << 23);
    {
        const float ar = -rbf(__expf(bf2f(r_lar)));
        const float zr = ar * dt, zi = bf2f(r_aim) * dt;
        const bool isU = w >= 2;
        const float pb = isU ? 16.0f : 1.0f;
        const float zr2 = zr * 1.4426950408889634f;
        const float zi2 = zi * (isU ? -0.15915494309189535f : 0.15915494309189535f);
        cplx e[8];
#pragma unroll
        for (int k = 0; k < 8; ++k) e[k] = cexp_p(zr2, zi2, pb * (float)(1 << k));
        cplx v[41];
        {
            const float crv = bf2f((w & 1) ? r_c2 : r_c0), civ = bf2f((w & 1) ? r_c3 : r_c1);
            v[0] = isU ? cplx{crv * dt * sc, -(civ * dt * sc)} : cplx{1.0f, 0.0f};
        }
        char* tb = (isU ? Ut : Vt) + n * 4;
        auto put = [&](int i0, int i1) {
#pragma unroll
            for (int i = i0; i < i1; ++i) *(unsigned*)(tb + i * CV_RS) = pack_h(v[i].r, v[i].i);
            SCHED_FENCE();
        };
        v[1] = cmul(v[0], e[0]);
#pragma unroll
        for (int k = 0; k < 2; ++k) v[2 + k] = cmul(v[k], e[1]);
#pragma unroll
        for (int k = 0; k < 4; ++k) v[4 + k] = cmul(v[k], e[2]);
#pragma unroll
        for (int k = 0; k < 8; ++k) v[8 + k] = cmul(v[k], e[3]);
        v[40] = cmul(v[0], e[7]);
        if (isU) *(unsigned*)(tb + 40 * CV_RS) = pack_h(v[40].r, v[40].i);
        put(0, 16);
#pragma unroll
        for (int k = 0; k < 8; ++k) v[16 + k] = cmul(v[2 * k], e[4]);
        put(16, 24);
#pragma unroll
        for (int k = 0; k < 8; ++k) v[24 + k] = cmul(v[k < 4 ? 4 * k : 16 + (4 * k - 16) / 2], e[5]);
        put(24, 32);
#pragma unroll
        for (int k = 0; k < 8; ++k) {
            const int ee = 8 * k;
            const int src = ee < 16 ? ee : (ee < 32 ? 16 + (ee - 16) / 2 : 24 + (ee - 32) / 4);
            v[32 + k] = cmul(v[src], e[6]);
        }
        put(32, 40);
    }
    __syncthreads();
    const int j = lane & 15, q = lane >> 4;
    {
        const int urow = (w == 0) ? j : (8 + 8 * w + j);
        const int vrow = bset_idx(j << w);
        const char* ua = Ut + urow * CV_RS + q * 16;
        const char* vb = Vt + vrow * CV_RS + q * 16;
        f16x8 a[8], bb[8];
#pragma unroll
        for (int ks = 0; ks < 8; ++ks) { a[ks] = *(const f16x8*)(ua + ks * 64); bb[ks] = *(const f16x8*)(vb + ks * 64); }
        SCHED_FENCE();
        f32x4 acc0 = {0.f, 0.f, 0.f, 0.f}, acc1 = {0.f, 0.f, 0.f, 0.f};
#pragma unroll
        for (int ks = 0; ks < 8; ks += 2) {
            acc0 = __builtin_amdgcn_mfma_f32_16x16x32_f16(a[ks], bb[ks], acc0, 0, 0, 0);
            acc1 = __builtin_amdgcn_mfma_f32_16x16x32_f16(a[ks + 1], bb[ks + 1], acc1, 0, 0, 0);
        }
        u32x2 o = {pack_h(acc0[0] + acc1[0], acc0[1] + acc1[1]), pack_h(acc0[2] + acc1[2], acc0[3] + acc1[3])};
        gstore((u16*)(ws + WS_KD) + (size_t)d * 1024 + w * 256 + j * 16 + 4 * q, o);
        if (tid == 0) ((float*)(ws + WS_KSC))[d] = 2.0f / sc;
    }
}

__global__ __launch_bounds__(256) void k0_main(const u16* __restrict__ x, const u16* __restrict__ wg,
                                               const u16* __restrict__ wu, const u16* __restrict__ wd,
                                               const u16* __restrict__ lar, const u16* __restrict__ aim,
                                               const u16* __restrict__ cre, const u16* __restrict__ cim,
                                               const u16* __restrict__ ldt, char* __restrict__ ws) {
    __shared__ __attribute__((aligned(16))) char smem[K0_SMEM];
    unsigned pf = code_prefetch<K0_CODE>(threadIdx.x);
    {
        unsigned long long km;
        asm volatile("s_getpc_b64 s[20:21]\n\ts_add_u32 s20, s20, _Z7km_mainPKtS0_PcPt@rel32@lo+4\n\ts_addc_u32 s21, s21, _Z7km_mainPKtS0_PcPt@rel32@hi+12\n\ts_mov_b64 %0, s[20:21]"
                     : "=s"(km) :: "s20", "s21", "scc");
        if ((blockIdx.x & 63) < 8 && threadIdx.x * 128 < KM_CODE) pf ^= *(const volatile unsigned*)((const char*)km + threadIdx.x * 128);
    }
    int b = blockIdx.x;
    STAMP(ws, 0, 0);
    if (b < K0_KG) {
        k0_kgen(b, lar, aim, cre, cim, ldt, ws, smem);
    } else if (b < K0_KG + K0_LN) {
        __builtin_amdgcn_s_setprio(K0_PRIO);
        k0_ln(b - K0_KG, x, (u16*)(ws + WS_XNF), (u16*)(ws + WS_XNT), (f32x2*)(ws + WS_STAT), smem);
    } else {
        __builtin_amdgcn_s_setprio(K0_PRIO);
        b -= K0_KG + K0_LN;
        if (b == 0) {
#pragma unroll
            for (int i = 0; i < 48; ++i) ((unsigned*)(ws + WS_FLAG))[threadIdx.x + 256 * i] = 0u;
        }
        k0_w1(b, wg, wu, (u16*)(ws + WS_W1), wd, (u16*)(ws + WS_W2), smem);
    }
    CODE_PF_DONE(pf);
    STAMP(ws, 0, 3);
}

#define CV_SLEEP 8
#define G_PRIO 2
#define CV_TRIG 1u
constexpr int KM_CV = 256, KM_G = 256;
constexpr int G1_STAGE = 22 * 1024;
constexpr int CV_COPY = 4160;
constexpr int CV_XOFF = 8 * CV_COPY;
constexpr int CV_KD = CV_XOFF + 4608;
constexpr int CV_CH = 39968;
constexpr int KM_SMEM = 2 * CV_CH;

__device__ __forceinline__ float gelu_tanh(float x) {
    const float t = x * (-2.302208198f - 0.1029432397f * x * x);
    return x * __builtin_amdgcn_rcpf(1.0f + __builtin_amdgcn_exp2f(t));
}
__device__ __forceinline__ void glds16(unsigned ldsaddr, unsigned voff, const void* sbase) {
    asm volatile("s_mov_b32 m0, %0\n\ts_nop 0\n\tglobal_load_lds_dwordx4 %1, %2"
                 :: "s"(ldsaddr), "v"(voff), "s"(sbase) : "memory");
}
#define GLDS_OFF(voff, sbase, off) asm volatile("global_load_lds_dwordx4 %0, %1 offset:%2" :: "v"(voff), "s"(sbase), "i"(off) : "memory")
#define DS_RD128(dst, addr, off) asm volatile("ds_read_b128 %0, %1 offset:%2" : "=v"(dst) : "v"(addr), "i"(off))
typedef __attribute__((ext_vector_type(16))) float f32x16;

template <int NT>
__device__ __forceinline__ void km_gemm1_wave(const int w, const int lane, const int mb, const int nb, char* smem, char* ws) {
    const int lg = w & 3;
    constexpr int tl0 = NT == 4 ? 0 : 4;
    const unsigned lds0 = (unsigned)(uintptr_t)(LAS char*)smem;
    const int wfr = NT == 4 ? 3 * lg : (lg == 0 ? 12 : 13);
    const char* wbase = ws + WS_W1 + ((size_t)nb * 16 * 14 + wfr) * 1024;
    const char* xbase = ws + WS_XNF + ((size_t)(mb * 4 + lg) * 16) * 2048;
    const unsigned l16 = lane * 16;
    const unsigned wdst = lds0 + wfr * 1024, xdst = lds0 + (14 + 2 * lg) * 1024;
    auto stage = [&](int ks, unsigned boff) {
        const char* wb = wbase + ks * 14336;
        glds16(wdst + boff, l16, wb);
        if constexpr (NT == 4) {
            GLDS_OFF(l16, wb, 1024);
            GLDS_OFF(l16, wb, 2048);
        } else {
            const char* xb = xbase + ks * 2048;
            glds16(xdst + boff, l16, xb);
            GLDS_OFF(l16, xb, 1024);
        }
    };
    const unsigned aw = lds0 + tl0 * 1024 + l16, ax = lds0 + (14 + 2 * lg) * 1024 + l16;
    f32x16 acc[NT];
#pragma unroll
    for (int t = 0; t < NT; ++t)
#pragma unroll
        for (int i = 0; i < 16; ++i) acc[t][i] = 0.f;
    u32x4 wf[2][NT], xf[2];
    stage(0, 0);
    stage(1, G1_STAGE);
    stage(2, 2 * G1_STAGE);
    asm volatile("s_waitcnt vmcnt(6)" ::: "memory");
    __builtin_amdgcn_s_barrier();
    SCHED_FENCE();
    STAMP(ws, 1, 1);
    DS_RD128(xf[0], ax, 0);
#pragma unroll
    for (int t = 0; t < NT; ++t) DS_RD128(wf[0][t], aw, t * 1024);
    DS_RD128(xf[1], ax, 1024);
#pragma unroll
    for (int t = 0; t < NT; ++t) DS_RD128(wf[1][t], aw, 7168 + t * 1024);
    SCHED_FENCE();
    auto iter = [&](int ks, auto bufc, auto modec) {
        constexpr int BUF = decltype(bufc)::value;
        constexpr int MODE = decltype(modec)::value;
        constexpr int NB = ((BUF + 1) % 3) * G1_STAGE;
#pragma unroll
        for (int t = 0; t < NT; ++t) {
            if (t == 0) asm volatile("s_waitcnt lgkmcnt(%3)" : "+v"(wf[0][0]), "+v"(wf[0][1]), "+v"(xf[0]) : "i"(2 * NT - 1));
            if (t == 2) {
                if constexpr (NT == 4) asm volatile("s_waitcnt lgkmcnt(%2)" : "+v"(wf[0][2]), "+v"(wf[0][NT - 1]) : "i"(NT + 1));
                else asm volatile("s_waitcnt lgkmcnt(%1)" : "+v"(wf[0][2]) : "i"(NT + 1));
            }
            acc[t] = __builtin_amdgcn_mfma_f32_32x32x16_bf16(__builtin_bit_cast(bf16x8, wf[0][t]), __builtin_bit_cast(bf16x8, xf[0]), acc[t], 0, 0, 0);
            SCHED_FENCE();
        }
        if constexpr (NT == 4) asm volatile("s_waitcnt lgkmcnt(0)" : "+v"(xf[1]), "+v"(wf[1][0]), "+v"(wf[1][1]), "+v"(wf[1][2]), "+v"(wf[1][NT - 1]) :: "memory");
        else asm volatile("s_waitcnt lgkmcnt(0)" : "+v"(xf[1]), "+v"(wf[1][0]), "+v"(wf[1][1]), "+v"(wf[1][2]) :: "memory");
        if (MODE < 3) {
            if (MODE < 2) asm volatile("s_waitcnt vmcnt(3)" ::: "memory");
            else asm volatile("s_waitcnt vmcnt(0)" ::: "memory");
            SCHED_FENCE();
            __builtin_amdgcn_s_barrier();
        }
        SCHED_FENCE();
#pragma unroll
        for (int t = 0; t < NT; ++t) {
            acc[t] = __builtin_amdgcn_mfma_f32_32x32x16_bf16(__builtin_bit_cast(bf16x8, wf[1][t]), __builtin_bit_cast(bf16x8, xf[1]), acc[t], 0, 0, 0);
            SCHED_FENCE();
            if (MODE < 3 && t == 0) {
                DS_RD128(xf[0], ax, NB);
#pragma unroll
                for (int u = 0; u < NT; ++u) DS_RD128(wf[0][u], aw, NB + u * 1024);
            }
            if (MODE == 0 && t == 1) stage(ks + 3, BUF * G1_STAGE);
            SCHED_FENCE();
        }
        if (MODE < 3) {
            DS_RD128(xf[1], ax, NB + 1024);
#pragma unroll
            for (int t = 0; t < NT; ++t) DS_RD128(wf[1][t], aw, NB + 7168 + t * 1024);
        }
        SCHED_FENCE();
    };
    using I0 = std::integral_constant<int, 0>; using I1 = std::integral_constant<int, 1>;
    using I2 = std::integral_constant<int, 2>; using I3 = std::integral_constant<int, 3>;
#pragma unroll 1
    for (int k3 = 0; k3 < 12; k3 += 3) {
        iter(k3, I0{}, I0{});
        iter(k3 + 1, I1{}, I0{});
        iter(k3 + 2, I2{}, I0{});
    }
    iter(12, I0{}, I0{});
    iter(13, I1{}, I1{});
    iter(14, I2{}, I2{});
    iter(15, I0{}, I3{});
    __builtin_amdgcn_s_barrier();
    if (NT == 4 && w == 0 && lane < 4)
        __hip_atomic_fetch_add((GAS unsigned*)(ws + WS_FLAG) + 64 * (64 + 8 * mb + 4 + lane), 1u, __ATOMIC_RELAXED, __HIP_MEMORY_SCOPE_AGENT);
    STAMP(ws, 1, 2);
    const int hh = lane >> 5;
    const int lt = 8 * mb + 2 * lg + ((lane >> 4) & 1);
#pragma unroll
    for (int t = 0; t < NT; ++t) {
        const int T = 7 * nb + tl0 + t;
        u32x4 p;
#pragma unroll
        for (int i2 = 0; i2 < 4; ++i2) {
            float hv[2];
#pragma unroll
            for (int r = 0; r < 2; ++r) {
                const float g = rbf(acc[t][2 * i2 + r]), u = rbf(acc[t][8 + 2 * i2 + r]);
                hv[r] = rbf(gelu_tanh(g)) * u;
            }
            p[i2] = pack_bf(hv[0], hv[1]);
        }
        gstore(ws + WS_HF + ((size_t)(lt * 56 + (T >> 1)) * 64 + (2 * (T & 1) + hh) * 16 + (lane & 15)) * 16, p);
    }
}

struct IdxTab { u16 v[2064]; };
constexpr IdxTab make_idx_tab() {
    IdxTab t{};
    for (int i = 0; i < 2064; ++i) {
        int lag = 2047 - i;
        if (lag < 0) { t.v[i] = 1024; continue; }
        int p = lag;
        if (lag >= 256) {
            int s = 0;
            while ((lag >> s) >= 256) ++s;
            int step = 1 << s, rem = lag & (step - 1), base = lag - rem, half = step >> 1;
            if (rem > half || (rem == half && ((base >> s) & 1))) base += step;
            p = base;
        }
        int e = 0, r = p;
        if (p >= 256) {
            while ((p >> e) >= 256) ++e;
            if (e > 3) e = 3;
            r = (p - (128 << e)) >> e;
        }
        t.v[i] = (u16)(e * 256 + (r & 15) * 16 + (r >> 4));
    }
    return t;
}
__device__ const IdxTab kIdxTab = make_idx_tab();

__device__ __forceinline__ void km_conv(const int cb, char* __restrict__ ws, char* smem0) {
    const int tid = threadIdx.x & 255, lane = tid & 63;
    const int w8 = __builtin_amdgcn_readfirstlane(threadIdx.x >> 6);
    const int ch = w8 >> 2, w = w8 & 3;
    const int d = 2 * cb + ch;
    char* smem = smem0 + ch * CV_CH;
    u16* Kd = (u16*)(smem + CV_KD);
    const u32x2 kdv = *(const u32x2*)((const u16*)(ws + WS_KD) + (size_t)d * 1024 + tid * 4);
    const float osc = ((const float*)(ws + WS_KSC))[d];
    const u32x4 it0 = *(const u32x4*)(kIdxTab.v + 8 * tid), it1 = *(const u32x4*)(kIdxTab.v + 8 * tid + 8);
    const u32x4 xrow = *(const u32x4*)((const u16*)(ws + WS_XNT) + (size_t)d * L_ + tid * 8);
    SCHED_FENCE();
    *(u32x2*)(Kd + tid * 4) = kdv;
    if (tid == 0) Kd[1024] = 0;
    if (w8 == 0) {
        GAS unsigned* cnt = (GAS unsigned*)(ws + WS_FLAG) + 64 * (64 + 8 * (cb & 15) + 4 + ((cb >> 4) & 3));
        for (unsigned spins = 0; spins < 128; ++spins) {
            if (__builtin_amdgcn_readfirstlane(__hip_atomic_load(cnt, __ATOMIC_RELAXED, __HIP_MEMORY_SCOPE_AGENT)) >= CV_TRIG) break;
            __builtin_amdgcn_s_sleep(CV_SLEEP);
        }
    }
    __syncthreads();
    STAMP(ws, 1, 1);
    const int j = lane & 15, q = lane >> 4;
    {
        unsigned wd[8];
#pragma unroll
        for (int e = 0; e < 4; ++e) {
            wd[e] = (unsigned)Kd[it0[e] & 0xffff] | ((unsigned)Kd[it0[e] >> 16] << 16);
            wd[4 + e] = (unsigned)Kd[it1[e] & 0xffff] | ((unsigned)Kd[it1[e] >> 16] << 16);
        }
        char* dst = smem + tid * 16;
#pragma unroll
        for (int e = 0; e < 4; ++e) {
            u32x4 ev = {wd[e], wd[e + 1], wd[e + 2], wd[e + 3]};
            *(u32x4*)(dst + (2 * e) * CV_COPY) = ev;
            u32x4 od;
#pragma unroll
            for (int k = 0; k < 4; ++k) od[k] = __builtin_amdgcn_alignbit(wd[e + k + 1], wd[e + k], 16);
            *(u32x4*)(dst + (2 * e + 1) * CV_COPY) = od;
        }
        char* xp = smem + CV_XOFF;
        if (tid < 32) *(u32x4*)(xp + tid * 16) = u32x4{0u, 0u, 0u, 0u};
        if (tid >= 64 && tid < 72) *(u32x4*)(smem + (tid - 64) * CV_COPY + 256 * 16) = u32x4{0u, 0u, 0u, 0u};
        *(u32x4*)(xp + 512 + tid * 16) = xrow;
    }
    __syncthreads();
    STAMP(ws, 1, 2);
    const char* kb = smem + ((7 - j) & 7) * CV_COPY + (253 + q - (j >> 3)) * 16 - w * 2 * 64;
    const char* xq = smem + CV_XOFF + (256 + 16 * j - 16 + 8 * q) * 2 - w * 2 * 64;
    f32x4 acc[8];
#pragma unroll
    for (int i = 0; i < 8; ++i) acc[i] = f32x4{0.f, 0.f, 0.f, 0.f};
    {
        f16x8 bfr[2][8], af[3];
#pragma unroll
        for (int c = 0; c < 8; ++c) bfr[0][c] = *(const f16x8*)(xq + c * 512);
        af[0] = *(const f16x8*)(kb);
        af[1] = *(const f16x8*)(kb - 8 * 64);
        SCHED_FENCE();
#pragma unroll
        for (int ri = 0; ri < 2; ++ri) {
#pragma unroll
            for (int mm = 0; mm < 8; ++mm) {
                const int g = ri * 8 + mm;
                if (g + 2 < 16) {
                    const int g2 = g + 2;
                    af[g2 % 3] = *(const f16x8*)(kb - (8 * (g2 & 7) + (g2 >> 3)) * 64);
                }
                if (ri < 1) bfr[1][mm] = *(const f16x8*)(xq + mm * 512 - 64);
#pragma unroll
                for (int ct = mm; ct < 8; ++ct)
                    acc[ct] = __builtin_amdgcn_mfma_f32_16x16x32_f16(af[g % 3], bfr[ri][ct - mm], acc[ct], 0, 0, 0);
                SCHED_FENCE();
            }
        }
    }
    STAMP(ws, 1, 4);
    __syncthreads();
    {
        f32x4* exb = (f32x4*)(smem + w * 8192) + lane;
#pragma unroll
        for (int i = 0; i < 8; ++i) exb[i * 64] = acc[i];
    }
    __syncthreads();
    {
        u16* yp = (u16*)(ws + WS_YT) + (size_t)d * L_ + 16 * j + 4 * q + 512 * w;
#pragma unroll
        for (int i = 0; i < 2; ++i) {
            const f32x4* exb = (const f32x4*)(smem + (2 * w + i) * 1024) + lane;
            f32x4 o = exb[0] + exb[512] + exb[1024] + exb[1536];
            u32x2 p = {pack_bf(o[0] * osc, o[1] * osc), pack_bf(o[2] * osc, o[3] * osc)};
            gstore(yp + 256 * i, p);
        }
    }
    STAMP(ws, 1, 6);
    asm volatile("s_waitcnt vmcnt(0)" ::: "memory");
    __syncthreads();
    if (threadIdx.x < 8) __hip_atomic_fetch_add((GAS unsigned*)(ws + WS_FLAG) + 64 * (8 * (cb >> 5) + threadIdx.x), 1u, __ATOMIC_RELAXED, __HIP_MEMORY_SCOPE_AGENT);
}

struct G2Map { int lb, db; };
__device__ __forceinline__ G2Map g2_map(int bid) { const int xcd = bid & 7, i5 = bid >> 3; return G2Map{4 * xcd + (i5 & 3), i5 >> 2}; }
__device__ __forceinline__ void g2_stage_w(const int bid, const int w8, const int lane, const int k, char* ws, char* smem) {
    const int kg = w8 & 3, lh = w8 >> 2;
    const unsigned lds0 = (unsigned)(uintptr_t)(LAS char*)smem;
    const char* src = ws + WS_W2 + ((size_t)(g2_map(bid).db * 56 + 14 * kg + k) * 4 + 2 * lh) * 1024;
    const unsigned l16 = lane * 16;
    glds16(lds0 + (k & 3) * 16384 + kg * 4096 + lh * 2048, l16, src);
    GLDS_OFF(l16, src, 1024);
}
#define HLOAD(dst, voff, base, off) asm volatile("global_load_dwordx4 %0, %1, %2 offset:%3 sc1" : "=v"(dst) : "v"(voff), "s"(base), "i"(off))
__device__ __forceinline__ void km_gemm2(const int bid, const int w8, const int lane, const u16* __restrict__ x,
                                         const u16* __restrict__ dskip, char* __restrict__ ws, u16* __restrict__ out, char* smem) {
    const int kg = w8 & 3, lh = w8 >> 2;
    const int xcd = bid & 7;
    const int lb = g2_map(bid).lb, db = g2_map(bid).db;
    const int r = lane & 15, q = lane >> 4;
    const __amdgpu_buffer_rsrc_t yrs = __builtin_amdgcn_make_buffer_rsrc(ws + WS_YT, 0, D_ * L_ * 2, 0x00020000);
    const int l16 = lane * 16;
    const char* hb0 = ws + WS_HF + (size_t)((4 * lb + 2 * lh) * 56 + 14 * kg) * 1024;
    const char* hb1 = hb0 + 56 * 1024;
    const unsigned awr = (unsigned)(uintptr_t)(LAS char*)smem + kg * 4096 + l16;
    if (w8 == 0) {
        GAS unsigned* cnt = (GAS unsigned*)(ws + WS_FLAG) + 64 * (64 + 8 * (lb >> 1) + (db & 3));
        for (unsigned spins = 0; spins < (1u << 22); ++spins) {
            if (__builtin_amdgcn_readfirstlane(__hip_atomic_load(cnt, __ATOMIC_RELAXED, __HIP_MEMORY_SCOPE_AGENT)) == 16u) break;
            __builtin_amdgcn_s_sleep(4);
        }
    }
    __syncthreads();
    STAMP(ws, 1, 5);
    u32x4 hf[3][2], wf[4];
#pragma unroll
    for (int s = 0; s < 3; ++s) { HLOAD(hf[s][0], l16, hb0, s * 1024); HLOAD(hf[s][1], l16, hb1, s * 1024); }
    SCHED_FENCE();
    f32x4 acc[4][2];
#pragma unroll
    for (int t = 0; t < 4; ++t)
#pragma unroll
        for (int i = 0; i < 2; ++i) acc[t][i] = f32x4{0.f, 0.f, 0.f, 0.f};
    const int oi = kg >> 1, ot = 2 * (kg & 1);
    const int lrow = 64 * lb + 32 * lh + 16 * oi + r;
    const int dcol = 64 * db + 16 * q + 4 * ot;
    auto poll = [&]() {
        return __hip_atomic_load((GAS unsigned*)(ws + WS_FLAG) + 64 * (8 * db + xcd), __ATOMIC_RELAXED, __HIP_MEMORY_SCOPE_AGENT);
    };
    auto ldy = [&]() {
        int ln = l16; asm volatile("" : "+v"(ln));
        const int row = ln >> 5;
        const int yo = (16 * (row >> 3) + (row & 7)) * (L_ * 2) + (ln & 16);
        const int so = ((64 * db + 4 * ot) * L_ + 64 * lb + 32 * lh + 16 * oi) * 2;
        return __builtin_amdgcn_raw_buffer_load_b128(yrs, yo, so, 16);
    };
    unsigned fv = 0;
    bool early = false;
    u32x4 yv, xv, dv;
    f32x2 st;
#pragma unroll
    for (int k = 0; k < 14; ++k) {
        const int s = k % 3;
        constexpr int kN[14] = {4, 6, 8, 8, 8, 8, 8, 8, 8, 8, 8, 8, 4, 0};
        asm volatile("s_waitcnt vmcnt(%2)" : "+v"(hf[s][0]), "+v"(hf[s][1]) : "i"(kN[k]) : "memory");
        __builtin_amdgcn_s_barrier();
        SCHED_FENCE();
#pragma unroll
        for (int t = 0; t < 4; ++t) DS_RD128(wf[t], awr, (k & 3) * 16384 + t * 1024);
#pragma unroll
        for (int t = 0; t < 4; ++t) {
            asm volatile("s_waitcnt lgkmcnt(%1)" : "+v"(wf[t]) : "i"(3 - t));
#pragma unroll
            for (int i = 0; i < 2; ++i)
                acc[t][i] = __builtin_amdgcn_mfma_f32_16x16x32_bf16(__builtin_bit_cast(bf16x8, wf[t]), __builtin_bit_cast(bf16x8, hf[s][i]), acc[t][i], 0, 0, 0);
            SCHED_FENCE();
        }
        if (k + 3 < 14) {
            g2_stage_w(bid, w8, lane, k + 3, ws, smem);
            const char* h0 = hb0 + (k + 3) * 1024;
            const char* h1 = hb1 + (k + 3) * 1024;
            HLOAD(hf[s][0], l16, h0, 0);
            HLOAD(hf[s][1], l16, h1, 0);
        }
        if (k == 11) fv = poll();
        if (k == 13) {
            early = __builtin_amdgcn_readfirstlane(fv) == 32u;
            if (early) yv = ldy();
            xv = *(const u32x4*)(x + (size_t)lrow * D_ + dcol);
            dv = *(const u32x4*)(dskip + dcol);
            st = ((const f32x2*)(ws + WS_STAT))[lrow];
        }
        SCHED_FENCE();
    }
    __builtin_amdgcn_s_barrier();
    STAMP(ws, 1, 6);
    {
        f32x4* red = (f32x4*)smem + lh * (24 * 64);
#pragma unroll
        for (int dk = 0; dk < 4; ++dk) {
            if (dk != kg) {
                const int si = kg < dk ? kg : kg - 1;
#pragma unroll
                for (int tt = 0; tt < 2; ++tt) red[((dk * 3 + si) * 2 + tt) * 64 + lane] = acc[2 * (dk & 1) + tt][dk >> 1];
            }
        }
    }
    if (w8 == 0 && !early) {
        for (unsigned spins = 0; spins < (1u << 22); ++spins) {
            if (__builtin_amdgcn_readfirstlane(poll()) == 32u) break;
            __builtin_amdgcn_s_sleep(8);
        }
    }
    __syncthreads();
    if (!early) yv = ldy();
    STAMP(ws, 1, 7);
    f32x4 f[2];
    {
        const f32x4* red = (const f32x4*)smem + lh * (24 * 64);
#pragma unroll
        for (int tt = 0; tt < 2; ++tt)
            f[tt] = kg == 0 ? acc[tt][0] : (kg == 1 ? acc[2 + tt][0] : (kg == 2 ? acc[tt][1] : acc[2 + tt][1]));
#pragma unroll
        for (int si = 0; si < 3; ++si)
#pragma unroll
            for (int tt = 0; tt < 2; ++tt) f[tt] += red[((kg * 3 + si) * 2 + tt) * 64 + lane];
    }
    u16 yt[8];
    {
        char* yb = smem + lh * 24576 + kg * 6144;
        asm volatile("" ::: "memory");
        *(u32x4*)(yb + lane * 16) = yv;
        asm volatile("s_waitcnt lgkmcnt(0)" ::: "memory");
        const u16* yr = (const u16*)(yb + q * 256 + 2 * r);
#pragma unroll
        for (int e = 0; e < 8; ++e) yt[e] = yr[e * 16];
    }
    u32x4 ov;
#pragma unroll
    for (int jj = 0; jj < 4; ++jj) {
        float res[2];
#pragma unroll
        for (int hh = 0; hh < 2; ++hh) {
            const int e = 2 * jj + hh;
            const float fv2 = rbf(f[e >> 2][e & 3]);
            const float g = rbf(fv2 * __builtin_amdgcn_rcpf(1.0f + __expf(-fv2)));
            const float ds = hh ? bfhi(dv[jj]) : bflo(dv[jj]);
            const float xx = hh ? bfhi(xv[jj]) : bflo(xv[jj]);
            const float xn = rbf(rbf(xx - st[0]) * st[1]);
            const float s = rbf(bf2f(yt[e]) + rbf(xn * ds));
            res[hh] = xx + rbf(s * g);
        }
        ov[jj] = pack_bf(res[0], res[1]);
    }
    *(u32x4*)(out + (size_t)lrow * D_ + dcol) = ov;
}

__global__ __launch_bounds__(512, 4) void km_main(const u16* __restrict__ x, const u16* __restrict__ dskip,
                                                  char* __restrict__ ws, u16* __restrict__ out) {
    __shared__ __attribute__((aligned(16))) char smem[KM_SMEM];
    const int b = blockIdx.x;
    const unsigned pf = code_prefetch<KM_CODE>(threadIdx.x, KM_PF_ALL || b >= KM_G);
    STAMP(ws, 1, 0);
    if (b < KM_G) {
        const int lane = threadIdx.x & 63;
        const int w = __builtin_amdgcn_readfirstlane(threadIdx.x >> 6);
        const int xcd = b & 7, i5 = b >> 3;
        const int mb = (xcd & 1) * 8 + (i5 & 7), nb = (xcd >> 1) * 4 + (i5 >> 3);
        __builtin_amdgcn_s_setprio(G_PRIO);
        if (w < 4) km_gemm1_wave<4>(w, lane, mb, nb, smem, ws);
        else km_gemm1_wave<3>(w, lane, mb, nb, smem, ws);
#pragma unroll
        for (int k = 0; k < 3; ++k) g2_stage_w(b, w, lane, k, ws, smem);
        asm volatile("s_waitcnt vmcnt(6)" ::: "memory");
        __builtin_amdgcn_s_barrier();
        if (threadIdx.x < 4) __hip_atomic_fetch_add((GAS unsigned*)(ws + WS_FLAG) + 64 * (64 + 8 * mb + threadIdx.x), 1u, __ATOMIC_RELAXED, __HIP_MEMORY_SCOPE_AGENT);
        STAMP(ws, 1, 4);
        km_gemm2(b, w, lane, x, dskip, ws, out, smem);
        CODE_PF_DONE(pf);
    } else {
        km_conv(b - KM_G, ws, smem);
        CODE_PF_DONE(pf);
    }
    STAMP(ws, 1, 3);
}

extern "C" void kernel_launch(void* const* d_in, const int* in_sizes, int n_in,
                              void* d_out, int out_size, void* d_ws, size_t ws_size,
                              hipStream_t stream) {
    const u16* x   = (const u16*)d_in[0];
    const u16* lar = (const u16*)d_in[1];
    const u16* aim = (const u16*)d_in[2];
    const u16* cre = (const u16*)d_in[3];
    const u16* cim = (const u16*)d_in[4];
    const u16* ldt = (const u16*)d_in[5];
    const u16* dsk = (const u16*)d_in[6];
    const u16* wg  = (const u16*)d_in[8];
    const u16* wu  = (const u16*)d_in[9];
    const u16* wd  = (const u16*)d_in[10];
    char* ws = (char*)d_ws;
    k0_main<<<K0_GRID, 256, 0, stream>>>(x, wg, wu, wd, lar, aim, cre, cim, ldt, ws);
    km_main<<<KM_G + KM_CV, 512, 0, stream>>>(x, dsk, ws, (u16*)d_out);
}
```

```cpp
#include <hip/hip_runtime.h>
#include <hip/hip_bf16.h>
#include <stdint.h>
#include <type_traits>

typedef __attribute__((ext_vector_type(8))) __bf16 bf16x8;
typedef __attribute__((ext_vector_type(2))) __bf16 bf16x2;
typedef __attribute__((ext_vector_type(8))) _Float16 f16x8;
typedef __attribute__((ext_vector_type(4))) float f32x4;
typedef __attribute__((ext_vector_type(2))) float f32x2;
typedef __attribute__((ext_vector_type(4))) unsigned int u32x4;
typedef __attribute__((ext_vector_type(2))) unsigned int u32x2;
typedef unsigned short u16;
#define GAS __attribute__((address_space(1)))
#define LAS __attribute__((address_space(3)))

constexpr int L_ = 2048, D_ = 512, NS_ = 128, F_ = 1792;

constexpr size_t WS_XNT  = 0;
constexpr size_t WS_STAT = 2u << 20;
constexpr size_t WS_FLAG = (2u << 20) + 65536;
constexpr size_t WS_W2   = 3u << 20;
constexpr size_t WS_HF   = 5u << 20;
constexpr size_t WS_YT   = 13u << 20;
constexpr size_t WS_KD   = 15u << 20;
constexpr size_t WS_KSC  = 17u << 20;
constexpr size_t WS_XNF  = 18u << 20;
constexpr size_t WS_W1   = 20u << 20;

#define STAMP(ws_, kid, slot) do {} while (0)
#define SCHED_FENCE() __builtin_amdgcn_sched_barrier(0)

template <typename T> __device__ __forceinline__ void gstore(void* p, T v) {
    if constexpr (sizeof(T) == 16) asm volatile("global_store_dwordx4 %0, %1, off sc0 sc1\n\ts_nop 1" :: "v"(p), "v"(v));
    else asm volatile("global_store_dwordx2 %0, %1, off sc0 sc1\n\ts_nop 1" :: "v"(p), "v"(v));
}
__device__ __forceinline__ float bf2f(u16 v) { return __uint_as_float(((unsigned)v) << 16); }
__device__ __forceinline__ float bflo(unsigned v) { return __uint_as_float(v << 16); }
__device__ __forceinline__ float bfhi(unsigned v) { return __uint_as_float(v & 0xffff0000u); }
__device__ __forceinline__ u16 f2bf(float f) { __bf16 b = (__bf16)f; return __builtin_bit_cast(u16, b); }
__device__ __forceinline__ float rbf(float f) { return bf2f(f2bf(f)); }
__device__ __forceinline__ unsigned pack_bf(float lo, float hi) {
    return (unsigned)f2bf(lo) | ((unsigned)f2bf(hi) << 16);
}
__device__ __forceinline__ u16 f2h(float f) { _Float16 h = (_Float16)f; return __builtin_bit_cast(u16, h); }
typedef __attribute__((ext_vector_type(2))) _Float16 f16x2;
__device__ __forceinline__ unsigned pack_h(float lo, float hi) {
    return (unsigned)f2h(lo) | ((unsigned)f2h(hi) << 16);
}
__device__ __forceinline__ float wave_sum(float v) {
#pragma unroll
    for (int o = 32; o > 0; o >>= 1) v += __shfl_xor(v, o, 64);
    return v;
}
__device__ __forceinline__ float row16_sum(float v) {
    v += __builtin_bit_cast(float, __builtin_amdgcn_update_dpp(0, __builtin_bit_cast(int, v), 0x128, 0xf, 0xf, false));
    v += __builtin_bit_cast(float, __builtin_amdgcn_update_dpp(0, __builtin_bit_cast(int, v), 0x124, 0xf, 0xf, false));
    v += __builtin_bit_cast(float, __builtin_amdgcn_update_dpp(0, __builtin_bit_cast(int, v), 0x122, 0xf, 0xf, false));
    v += __builtin_bit_cast(float, __builtin_amdgcn_update_dpp(0, __builtin_bit_cast(int, v), 0x121, 0xf, 0xf, false));
    return v;
}
__device__ __forceinline__ float dot2bf(unsigned a, unsigned b, float c) {
    return __builtin_amdgcn_fdot2_f32_bf16(__builtin_bit_cast(bf16x2, a), __builtin_bit_cast(bf16x2, b), c, false);
}
template <int BYTES> __device__ __forceinline__ unsigned code_prefetch(int t, bool on = true) {
    unsigned acc = 0;
    unsigned long long pc;
    asm volatile("s_getpc_b64 %0" : "=s"(pc));
    const char* base = (const char*)(pc & ~(unsigned long long)127);
    if (on && t * 128 < BYTES) acc = *(const volatile unsigned*)(base + t * 128);
    return acc;
}
#define CODE_PF_DONE(v) asm volatile("" :: "v"(v))
__device__ __forceinline__ f32x2 ln_stats(float mean, float var) {
    const float mb = rbf(mean);
    const float sd = rbf(__builtin_sqrtf(rbf(rbf(var) + 1e-5f)));
    return f32x2{mb, 1.0f / sd};
}

#define K0_PRIO 1
constexpr int K0_W1 = 128, K0_LN = 128, K0_KG = 512;
constexpr int K0_GRID = K0_W1 + K0_LN + K0_KG;
constexpr int K0_SMEM = 44 * 1024;
constexpr int K0_CODE = 10240, KM_CODE = 16384;

__device__ __forceinline__ void k0_w2_load(int b, const u16* __restrict__ wd, u32x4 (&v)[4]) {
    const int tid = threadIdx.x, ks = b >> 1, dh = b & 1;
#pragma unroll
    for (int i = 0; i < 4; ++i) {
        int c = tid + 256 * i, r = c >> 5, ch = c & 31;
        v[i] = *(const u32x4*)(wd + (size_t)(32 * ks + r) * D_ + 256 * dh + ch * 8);
    }
}
__device__ __forceinline__ void k0_w2_put(const u32x4 (&v)[4], char* smem) {
    const int tid = threadIdx.x;
    u16* tile = (u16*)smem;
#pragma unroll
    for (int i = 0; i < 4; ++i) {
        int c = tid + 256 * i, r = c >> 5, ch = c & 31;
        *(u32x4*)(tile + r * 264 + ch * 8) = v[i];
    }
}
__device__ __forceinline__ void k0_w2_store(int b, u16* __restrict__ W2, const char* smem) {
    const int tid = threadIdx.x, ks = b >> 1, dh = b & 1;
    const u16* tile = (const u16*)smem;
#pragma unroll
    for (int dbl = 0; dbl < 4; ++dbl) {
        int t = tid >> 6, lane = tid & 63, r = lane & 15, q = lane >> 4;
        int dl = 64 * dbl + 16 * (r >> 2) + 4 * t + (r & 3);
        const u16* sp = tile + (16 * (q >> 1) + 4 * (q & 1)) * 264 + dl;
        u32x4 o4;
        o4[0] = (unsigned)sp[0 * 264] | ((unsigned)sp[1 * 264] << 16);
        o4[1] = (unsigned)sp[2 * 264] | ((unsigned)sp[3 * 264] << 16);
        o4[2] = (unsigned)sp[8 * 264] | ((unsigned)sp[9 * 264] << 16);
        o4[3] = (unsigned)sp[10 * 264] | ((unsigned)sp[11 * 264] << 16);
        gstore(W2 + ((size_t)((4 * dh + dbl) * 56 + ks) * 4 + t) * 512 + lane * 8, o4);
    }
}

__device__ __forceinline__ void k0_w1(int b, const u16* __restrict__ wg, const u16* __restrict__ wu,
                                      u16* __restrict__ W1, const u16* __restrict__ wd, u16* __restrict__ W2, char* smem) {
    const int nb = b >> 3, kp = b & 7, tid = threadIdx.x;
    u16* tile = (u16*)smem;
    u32x4 v[7];
#pragma unroll
    for (int i = 0; i < 7; ++i) {
        int c = tid + 256 * i;
        int gu = c / 896, rem = c - gu * 896, r = rem / 14, ch = rem - r * 14;
        v[i] = *(const u32x4*)((gu ? wu : wg) + (size_t)(64 * kp + r) * F_ + 112 * nb + ch * 8);
    }
    u32x4 wv[4];
    k0_w2_load(b < 112 ? b : 111, wd, wv);
    SCHED_FENCE();
#pragma unroll
    for (int i = 0; i < 7; ++i) {
        int c = tid + 256 * i;
        int gu = c / 896, rem = c - gu * 896, r = rem / 14, ch = rem - r * 14;
        *(u32x4*)(tile + (gu * 64 + r) * 120 + ch * 8) = v[i];
    }
    __syncthreads();
    u16* dst = W1 + (size_t)(nb * 16 + 2 * kp) * 14 * 512;
#pragma unroll
    for (int i = 0; i < 7; ++i) {
        int o = tid + 256 * i;
        int k16 = o / 448, rem = o - k16 * 448;
        int tl = rem >> 6, lane = rem & 63, fr = lane & 15, gu = (lane >> 4) & 1, h = lane >> 5;
        const u16* sp = tile + (gu * 64 + 16 * k16 + 8 * h) * 120 + 16 * tl + fr;
        u32x4 o4;
#pragma unroll
        for (int j = 0; j < 4; ++j) o4[j] = (unsigned)sp[(2 * j) * 120] | ((unsigned)sp[(2 * j + 1) * 120] << 16);
        gstore(dst + o * 8, o4);
    }
    __syncthreads();
    k0_w2_put(wv, smem);
    __syncthreads();
    if (b < 112) k0_w2_store(b, W2, smem);
}

__device__ __forceinline__ void k0_ln(int b, const u16* __restrict__ x, u16* __restrict__ xnF, u16* __restrict__ xnT,
                                      f32x2* __restrict__ stat, char* smem) {
    const int tid = threadIdx.x, w = tid >> 6, lane = tid & 63, rr = lane >> 4, c = lane & 15;
    constexpr int TS = 516;
    u16* tile = (u16*)smem;
    const int l0 = b * 16, rl = 4 * w + rr, row = l0 + rl;
    u32x4 raw[4];
#pragma unroll
    for (int i = 0; i < 4; ++i) raw[i] = *(const u32x4*)(x + (size_t)row * D_ + (c + 16 * i) * 8);
    float v[32];
#pragma unroll
    for (int i = 0; i < 4; ++i)
#pragma unroll
        for (int j = 0; j < 4; ++j) { v[8 * i + 2 * j] = bflo(raw[i][j]); v[8 * i + 2 * j + 1] = bfhi(raw[i][j]); }
    float s = 0.f;
#pragma unroll
    for (int j = 0; j < 32; ++j) s += v[j];
    const float mean = row16_sum(s) * (1.0f / D_);
    float q2 = 0.f;
#pragma unroll
    for (int j = 0; j < 32; ++j) { float dlt = v[j] - mean; q2 += dlt * dlt; }
    const f32x2 st = ln_stats(mean, row16_sum(q2) * (1.0f / D_));
    if (c == 0) gstore(stat + row, st);
#pragma unroll
    for (int i = 0; i < 4; ++i) {
        u32x4 pb, ph;
#pragma unroll
        for (int j = 0; j < 4; ++j) {
            pb[j] = pack_bf(rbf(v[8 * i + 2 * j] - st[0]) * st[1], rbf(v[8 * i + 2 * j + 1] - st[0]) * st[1]);
            ph[j] = pack_h(bflo(pb[j]), bfhi(pb[j]));
        }
        const int ch = c + 16 * i;
        gstore(xnF + ((size_t)((row >> 5) * 32 + (ch >> 1)) * 64 + (ch & 1) * 32 + (row & 31)) * 8, pb);
        u16* tp = tile + rl * TS + ch * 8;
        *(u32x2*)(tp) = u32x2{ph[0], ph[1]};
        *(u32x2*)(tp + 4) = u32x2{ph[2], ph[3]};
    }
    __syncthreads();
#pragma unroll
    for (int it = 0; it < 4; ++it) {
        int d = it * 128 + (tid >> 1), part = tid & 1;
        const u16* sp = tile + (8 * part) * TS + d;
        u32x4 o;
#pragma unroll
        for (int j = 0; j < 4; ++j) o[j] = (unsigned)sp[(2 * j) * TS] | ((unsigned)sp[(2 * j + 1) * TS] << 16);
        gstore(xnT + (size_t)d * L_ + l0 + 8 * part, o);
    }
}

__device__ __forceinline__ int bset_idx(int e) {
    if (e < 16) return e;
    int s = (31 - __builtin_clz(e)) - 3;
    return 8 + 8 * s + ((e - (8 << s)) >> s);
}
__device__ __forceinline__ unsigned pack_h2(float lo, float hi) {
    unsigned r;
    asm("v_cvt_pk_f16_f32 %0, %1, %2" : "=v"(r) : "v"(lo), "v"(hi));
    return r;
}
struct cplx { float r, i; };
__device__ __forceinline__ cplx cmul(cplx a, cplx b) { return cplx{a.r * b.r - a.i * b.i, a.r * b.i + a.i * b.r}; }
__device__ __forceinline__ cplx cexp_p(float zr2, float zi2, float p) {
    float mag = __builtin_amdgcn_exp2f(zr2 * p);
    float ph = __builtin_amdgcn_fractf(zi2 * p);
    return cplx{mag * __builtin_amdgcn_cosf(ph), mag * __builtin_amdgcn_sinf(ph)};
}

constexpr int CV_RS = 528;
__device__ __forceinline__ void k0_kgen(const int d, const u16* __restrict__ lar, const u16* __restrict__ aim,
                                        const u16* __restrict__ cre, const u16* __restrict__ cim,
                                        const u16* __restrict__ ldt, char* __restrict__ ws, char* smem) {
    const int tid = threadIdx.x, lane = tid & 63;
    const int w = __builtin_amdgcn_readfirstlane(tid >> 6);
    char* Ut = smem;
    char* Vt = smem + 41 * CV_RS;
    const int n = (w & 1) * 64 + lane;
    const u16 r_ldt = ldt[d];
    const u16 r_c0 = cre[d * NS_ + lane], r_c1 = cim[d * NS_ + lane], r_c2 = cre[d * NS_ + 64 + lane], r_c3 = cim[d * NS_ + 64 + lane];
    const u16 r_lar = lar[d * NS_ + n], r_aim = aim[d * NS_ + n];
    SCHED_FENCE();
    const float dt = rbf(__expf(bf2f(r_ldt)));
    float sabs;
    {
        const float rs = row16_sum(fabsf(bf2f(r_c0)) + fabsf(bf2f(r_c1)) + fabsf(bf2f(r_c2)) + fabsf(bf2f(r_c3)));
        const int ri = __builtin_bit_cast(int, rs);
        sabs = (__builtin_bit_cast(float, __builtin_amdgcn_readlane(ri, 0)) + __builtin_bit_cast(float, __builtin_amdgcn_readlane(ri, 16)) +
                __builtin_bit_cast(float, __builtin_amdgcn_readlane(ri, 32)) + __builtin_bit_cast(float, __builtin_amdgcn_readlane(ri, 48))) * dt;
    }
    int ex = (int)((__float_as_uint(sabs) >> 23) & 0xff);
    int se = 127 + 14 - (ex - 126);
    se = se < 1 ? 1 : (se > 254 ? 254 : se);
    const float sc = __uint_as_float((unsigned)se << 23);
    {
        const float ar = -rbf(__expf(bf2f(r_lar)));
        const float zr = ar * dt, zi = bf2f(r_aim) * dt;
        const bool isU = w >= 2;
        const float pb = isU ? 16.0f : 1.0f;
        const float zr2 = zr * 1.4426950408889634f;
        const float zi2 = zi * (isU ? -0.15915494309189535f : 0.15915494309189535f);
        cplx e[8];
#pragma unroll
        for (int k = 0; k < 8; ++k) e[k] = cexp_p(zr2, zi2, pb * (float)(1 << k));
        cplx v[41];
        {
            const float crv = bf2f((w & 1) ? r_c2 : r_c0), civ = bf2f((w & 1) ? r_c3 : r_c1);
            v[0] = isU ? cplx{crv * dt * sc, -(civ * dt * sc)} : cplx{1.0f, 0.0f};
        }
        char* tb = (isU ? Ut : Vt) + n * 4;
        auto put = [&](int i0, int i1) {
#pragma unroll
            for (int i = i0; i < i1; ++i) *(unsigned*)(tb + i * CV_RS) = pack_h2(v[i].r, v[i].i);
            SCHED_FENCE();
        };
        v[1] = cmul(v[0], e[0]);
#pragma unroll
        for (int k = 0; k < 2; ++k) v[2 + k] = cmul(v[k], e[1]);
#pragma unroll
        for (int k = 0; k < 4; ++k) v[4 + k] = cmul(v[k], e[2]);
#pragma unroll
        for (int k = 0; k < 8; ++k) v[8 + k] = cmul(v[k], e[3]);
        v[40] = cmul(v[0], e[7]);
        if (isU) *(unsigned*)(tb + 40 * CV_RS) = pack_h(v[40].r, v[40].i);
        put(0, 16);
#pragma unroll
        for (int k = 0; k < 8; ++k) v[16 + k] = cmul(v[2 * k], e[4]);
        put(16, 24);
#pragma unroll
        for (int k = 0; k < 8; ++k) v[24 + k] = cmul(v[k < 4 ? 4 * k : 16 + (4 * k - 16) / 2], e[5]);
        put(24, 32);
#pragma unroll
        for (int k = 0; k < 8; ++k) {
            const int ee = 8 * k;
            const int src = ee < 16 ? ee : (ee < 32 ? 16 + (ee - 16) / 2 : 24 + (ee - 32) / 4);
            v[32 + k] = cmul(v[src], e[6]);
        }
        put(32, 40);
    }
    __syncthreads();
    const int j = lane & 15, q = lane >> 4;
    {
        const int urow = (w == 0) ? j : (8 + 8 * w + j);
        const int vrow = bset_idx(j << w);
        const char* ua = Ut + urow * CV_RS + q * 16;
        const char* vb = Vt + vrow * CV_RS + q * 16;
        f16x8 a[8], bb[8];
#pragma unroll
        for (int ks = 0; ks < 8; ++ks) { a[ks] = *(const f16x8*)(ua + ks * 64); bb[ks] = *(const f16x8*)(vb + ks * 64); }
        SCHED_FENCE();
        f32x4 acc0 = {0.f, 0.f, 0.f, 0.f}, acc1 = {0.f, 0.f, 0.f, 0.f};
#pragma unroll
        for (int ks = 0; ks < 8; ks += 2) {
            acc0 = __builtin_amdgcn_mfma_f32_16x16x32_f16(a[ks], bb[ks], acc0, 0, 0, 0);
            acc1 = __builtin_amdgcn_mfma_f32_16x16x32_f16(a[ks + 1], bb[ks + 1], acc1, 0, 0, 0);
        }
        u32x2 o = {pack_h(acc0[0] + acc1[0], acc0[1] + acc1[1]), pack_h(acc0[2] + acc1[2], acc0[3] + acc1[3])};
        gstore((u16*)(ws + WS_KD) + (size_t)d * 1024 + w * 256 + j * 16 + 4 * q, o);
        if (tid == 0) ((float*)(ws + WS_KSC))[d] = 2.0f / sc;
    }
}

__global__ __launch_bounds__(256) void k0_main(const u16* __restrict__ x, const u16* __restrict__ wg,
                                               const u16* __restrict__ wu, const u16* __restrict__ wd,
                                               const u16* __restrict__ lar, const u16* __restrict__ aim,
                                               const u16* __restrict__ cre, const u16* __restrict__ cim,
                                               const u16* __restrict__ ldt, char* __restrict__ ws) {
    __shared__ __attribute__((aligned(16))) char smem[K0_SMEM];
    const unsigned pf = code_prefetch<K0_CODE>(threadIdx.x);
    int b = blockIdx.x;
    STAMP(ws, 0, 0);
    if (b < K0_KG) {
        k0_kgen(b, lar, aim, cre, cim, ldt, ws, smem);
    } else if (b < K0_KG + K0_LN) {
        __builtin_amdgcn_s_setprio(K0_PRIO);
        k0_ln(b - K0_KG, x, (u16*)(ws + WS_XNF), (u16*)(ws + WS_XNT), (f32x2*)(ws + WS_STAT), smem);
    } else {
        __builtin_amdgcn_s_setprio(K0_PRIO);
        b -= K0_KG + K0_LN;
        if (b == 0) {
#pragma unroll
            for (int i = 0; i < 48; ++i) ((unsigned*)(ws + WS_FLAG))[threadIdx.x + 256 * i] = 0u;
        }
        k0_w1(b, wg, wu, (u16*)(ws + WS_W1), wd, (u16*)(ws + WS_W2), smem);
    }
    CODE_PF_DONE(pf);
    STAMP(ws, 0, 3);
}

#define CV_SLEEP 8
#define G_PRIO 2
#define KM_PF_ALL 0
#define CV_TRIG 1u
constexpr int KM_CV = 256, KM_G = 256;
constexpr int G1_STAGE = 22 * 1024;
constexpr int CV_COPY = 4160;
constexpr int CV_XOFF = 8 * CV_COPY;
constexpr int CV_KD = CV_XOFF + 4608;
constexpr int CV_CH = 39968;
constexpr int KM_SMEM = 2 * CV_CH;

__device__ __forceinline__ float gelu_tanh(float x) {
    const float t = x * (-2.302208198f - 0.1029432397f * x * x);
    return x * __builtin_amdgcn_rcpf(1.0f + __builtin_amdgcn_exp2f(t));
}
__device__ __forceinline__ void glds16(unsigned ldsaddr, unsigned voff, const void* sbase) {
    asm volatile("s_mov_b32 m0, %0\n\ts_nop 0\n\tglobal_load_lds_dwordx4 %1, %2"
                 :: "s"(ldsaddr), "v"(voff), "s"(sbase) : "memory");
}
#define GLDS_OFF(voff, sbase, off) asm volatile("global_load_lds_dwordx4 %0, %1 offset:%2" :: "v"(voff), "s"(sbase), "i"(off) : "memory")
#define DS_RD128(dst, addr, off) asm volatile("ds_read_b128 %0, %1 offset:%2" : "=v"(dst) : "v"(addr), "i"(off))
typedef __attribute__((ext_vector_type(16))) float f32x16;

template <int NT>
__device__ __forceinline__ void km_gemm1_wave(const int w, const int lane, const int mb, const int nb, char* smem, char* ws) {
    const int lg = w & 3;
    constexpr int tl0 = NT == 4 ? 0 : 4;
    const unsigned lds0 = (unsigned)(uintptr_t)(LAS char*)smem;
    const int wfr = NT == 4 ? 3 * lg : (lg == 0 ? 12 : 13);
    const char* wbase = ws + WS_W1 + ((size_t)nb * 16 * 14 + wfr) * 1024;
    const char* xbase = ws + WS_XNF + ((size_t)(mb * 4 + lg) * 16) * 2048;
    const unsigned l16 = lane * 16;
    const unsigned wdst = lds0 + wfr * 1024, xdst = lds0 + (14 + 2 * lg) * 1024;
    auto stage = [&](int ks, unsigned boff) {
        const char* wb = wbase + ks * 14336;
        glds16(wdst + boff, l16, wb);
        if constexpr (NT == 4) {
            GLDS_OFF(l16, wb, 1024);
            GLDS_OFF(l16, wb, 2048);
        } else {
            const char* xb = xbase + ks * 2048;
            glds16(xdst + boff, l16, xb);
            GLDS_OFF(l16, xb, 1024);
        }
    };
    const unsigned aw = lds0 + tl0 * 1024 + l16, ax = lds0 + (14 + 2 * lg) * 1024 + l16;
    f32x16 acc[NT];
#pragma unroll
    for (int t = 0; t < NT; ++t)
#pragma unroll
        for (int i = 0; i < 16; ++i) acc[t][i] = 0.f;
    u32x4 wf[2][NT], xf[2];
    stage(0, 0);
    stage(1, G1_STAGE);
    stage(2, 2 * G1_STAGE);
    asm volatile("s_waitcnt vmcnt(6)" ::: "memory");
    __builtin_amdgcn_s_barrier();
    SCHED_FENCE();
    STAMP(ws, 1, 1);
    DS_RD128(xf[0], ax, 0);
#pragma unroll
    for (int t = 0; t < NT; ++t) DS_RD128(wf[0][t], aw, t * 1024);
    DS_RD128(xf[1], ax, 1024);
#pragma unroll
    for (int t = 0; t < NT; ++t) DS_RD128(wf[1][t], aw, 7168 + t * 1024);
    SCHED_FENCE();
    auto iter = [&](int ks, auto bufc, auto modec) {
        constexpr int BUF = decltype(bufc)::value;
        constexpr int MODE = decltype(modec)::value;
        constexpr int NB = ((BUF + 1) % 3) * G1_STAGE;
#pragma unroll
        for (int t = 0; t < NT; ++t) {
            if (t == 0) asm volatile("s_waitcnt lgkmcnt(%3)" : "+v"(wf[0][0]), "+v"(wf[0][1]), "+v"(xf[0]) : "i"(2 * NT - 1));
            if (t == 2) {
                if constexpr (NT == 4) asm volatile("s_waitcnt lgkmcnt(%2)" : "+v"(wf[0][2]), "+v"(wf[0][NT - 1]) : "i"(NT + 1));
                else asm volatile("s_waitcnt lgkmcnt(%1)" : "+v"(wf[0][2]) : "i"(NT + 1));
            }
            acc[t] = __builtin_amdgcn_mfma_f32_32x32x16_bf16(__builtin_bit_cast(bf16x8, wf[0][t]), __builtin_bit_cast(bf16x8, xf[0]), acc[t], 0, 0, 0);
            SCHED_FENCE();
        }
        if constexpr (NT == 4) asm volatile("s_waitcnt lgkmcnt(0)" : "+v"(xf[1]), "+v"(wf[1][0]), "+v"(wf[1][1]), "+v"(wf[1][2]), "+v"(wf[1][NT - 1]) :: "memory");
        else asm volatile("s_waitcnt lgkmcnt(0)" : "+v"(xf[1]), "+v"(wf[1][0]), "+v"(wf[1][1]), "+v"(wf[1][2]) :: "memory");
        if (MODE < 3) {
            if (MODE < 2) asm volatile("s_waitcnt vmcnt(3)" ::: "memory");
            else asm volatile("s_waitcnt vmcnt(0)" ::: "memory");
            SCHED_FENCE();
            __builtin_amdgcn_s_barrier();
        }
        SCHED_FENCE();
#pragma unroll
        for (int t = 0; t < NT; ++t) {
            acc[t] = __builtin_amdgcn_mfma_f32_32x32x16_bf16(__builtin_bit_cast(bf16x8, wf[1][t]), __builtin_bit_cast(bf16x8, xf[1]), acc[t], 0, 0, 0);
            SCHED_FENCE();
            if (MODE < 3 && t == 0) {
                DS_RD128(xf[0], ax, NB);
#pragma unroll
                for (int u = 0; u < NT; ++u) DS_RD128(wf[0][u], aw, NB + u * 1024);
            }
            if (MODE == 0 && t == 1) stage(ks + 3, BUF * G1_STAGE);
            SCHED_FENCE();
        }
        if (MODE < 3) {
            DS_RD128(xf[1], ax, NB + 1024);
#pragma unroll
            for (int t = 0; t < NT; ++t) DS_RD128(wf[1][t], aw, NB + 7168 + t * 1024);
        }
        SCHED_FENCE();
    };
    using I0 = std::integral_constant<int, 0>; using I1 = std::integral_constant<int, 1>;
    using I2 = std::integral_constant<int, 2>; using I3 = std::integral_constant<int, 3>;
#pragma unroll 1
    for (int k3 = 0; k3 < 12; k3 += 3) {
        iter(k3, I0{}, I0{});
        iter(k3 + 1, I1{}, I0{});
        iter(k3 + 2, I2{}, I0{});
    }
    iter(12, I0{}, I0{});
    iter(13, I1{}, I1{});
    iter(14, I2{}, I2{});
    iter(15, I0{}, I3{});
    __builtin_amdgcn_s_barrier();
    if (NT == 4 && w == 0 && lane < 4)
        __hip_atomic_fetch_add((GAS unsigned*)(ws + WS_FLAG) + 64 * (64 + 8 * mb + 4 + lane), 1u, __ATOMIC_RELAXED, __HIP_MEMORY_SCOPE_AGENT);
    STAMP(ws, 1, 2);
    const int hh = lane >> 5;
    const int lt = 8 * mb + 2 * lg + ((lane >> 4) & 1);
#pragma unroll
    for (int t = 0; t < NT; ++t) {
        const int T = 7 * nb + tl0 + t;
        u32x4 p;
#pragma unroll
        for (int i2 = 0; i2 < 4; ++i2) {
            float hv[2];
#pragma unroll
            for (int r = 0; r < 2; ++r) {
                const float g = rbf(acc[t][2 * i2 + r]), u = rbf(acc[t][8 + 2 * i2 + r]);
                hv[r] = rbf(gelu_tanh(g)) * u;
            }
            p[i2] = pack_bf(hv[0], hv[1]);
        }
        gstore(ws + WS_HF + ((size_t)(lt * 56 + (T >> 1)) * 64 + (2 * (T & 1) + hh) * 16 + (lane & 15)) * 16, p);
    }
}

struct IdxTab { u16 v[2064]; };
constexpr IdxTab make_idx_tab() {
    IdxTab t{};
    for (int i = 0; i < 2064; ++i) {
        int lag = 2047 - i;
        if (lag < 0) { t.v[i] = 1024; continue; }
        int p = lag;
        if (lag >= 256) {
            int s = 0;
            while ((lag >> s) >= 256) ++s;
            int step = 1 << s, rem = lag & (step - 1), base = lag - rem, half = step >> 1;
            if (rem > half || (rem == half && ((base >> s) & 1))) base += step;
            p = base;
        }
        int e = 0, r = p;
        if (p >= 256) {
            while ((p >> e) >= 256) ++e;
            if (e > 3) e = 3;
            r = (p - (128 << e)) >> e;
        }
        t.v[i] = (u16)(e * 256 + (r & 15) * 16 + (r >> 4));
    }
    return t;
}
__device__ const IdxTab kIdxTab = make_idx_tab();

__device__ __forceinline__ void km_conv(const int cb, char* __restrict__ ws, char* smem0) {
    const int tid = threadIdx.x & 255, lane = tid & 63;
    const int w8 = __builtin_amdgcn_readfirstlane(threadIdx.x >> 6);
    const int ch = w8 >> 2, w = w8 & 3;
    const int d = 2 * cb + ch;
    char* smem = smem0 + ch * CV_CH;
    u16* Kd = (u16*)(smem + CV_KD);
    const u32x2 kdv = *(const u32x2*)((const u16*)(ws + WS_KD) + (size_t)d * 1024 + tid * 4);
    const float osc = ((const float*)(ws + WS_KSC))[d];
    const u32x4 it0 = *(const u32x4*)(kIdxTab.v + 8 * tid), it1 = *(const u32x4*)(kIdxTab.v + 8 * tid + 8);
    const u32x4 xrow = *(const u32x4*)((const u16*)(ws + WS_XNT) + (size_t)d * L_ + tid * 8);
    SCHED_FENCE();
    *(u32x2*)(Kd + tid * 4) = kdv;
    if (tid == 0) Kd[1024] = 0;
    if (w8 == 0) {
        GAS unsigned* cnt = (GAS unsigned*)(ws + WS_FLAG) + 64 * (64 + 8 * (cb & 15) + 4 + ((cb >> 4) & 3));
        for (unsigned spins = 0; spins < 128; ++spins) {
            if (__builtin_amdgcn_readfirstlane(__hip_atomic_load(cnt, __ATOMIC_RELAXED, __HIP_MEMORY_SCOPE_AGENT)) >= CV_TRIG) break;
            __builtin_amdgcn_s_sleep(CV_SLEEP);
        }
    }
    __syncthreads();
    STAMP(ws, 1, 1);
    const int j = lane & 15, q = lane >> 4;
    {
        unsigned wd[8];
#pragma unroll
        for (int e = 0; e < 4; ++e) {
            wd[e] = (unsigned)Kd[it0[e] & 0xffff] | ((unsigned)Kd[it0[e] >> 16] << 16);
            wd[4 + e] = (unsigned)Kd[it1[e] & 0xffff] | ((unsigned)Kd[it1[e] >> 16] << 16);
        }
        char* dst = smem + tid * 16;
#pragma unroll
        for (int e = 0; e < 4; ++e) {
            u32x4 ev = {wd[e], wd[e + 1], wd[e + 2], wd[e + 3]};
            *(u32x4*)(dst + (2 * e) * CV_COPY) = ev;
            u32x4 od;
#pragma unroll
            for (int k = 0; k < 4; ++k) od[k] = __builtin_amdgcn_alignbit(wd[e + k + 1], wd[e + k], 16);
            *(u32x4*)(dst + (2 * e + 1) * CV_COPY) = od;
        }
        char* xp = smem + CV_XOFF;
        if (tid < 32) *(u32x4*)(xp + tid * 16) = u32x4{0u, 0u, 0u, 0u};
        if (tid >= 64 && tid < 72) *(u32x4*)(smem + (tid - 64) * CV_COPY + 256 * 16) = u32x4{0u, 0u, 0u, 0u};
        *(u32x4*)(xp + 512 + tid * 16) = xrow;
    }
    __syncthreads();
    STAMP(ws, 1, 2);
    const char* kb = smem + ((7 - j) & 7) * CV_COPY + (253 + q - (j >> 3)) * 16 - w * 2 * 64;
    const char* xq = smem + CV_XOFF + (256 + 16 * j - 16 + 8 * q) * 2 - w * 2 * 64;
    f32x4 acc[8];
#pragma unroll
    for (int i = 0; i < 8; ++i) acc[i] = f32x4{0.f, 0.f, 0.f, 0.f};
    {
        f16x8 bfr[2][8], af[3];
#pragma unroll
        for (int c = 0; c < 8; ++c) bfr[0][c] = *(const f16x8*)(xq + c * 512);
        af[0] = *(const f16x8*)(kb);
        af[1] = *(const f16x8*)(kb - 8 * 64);
        SCHED_FENCE();
#pragma unroll
        for (int ri = 0; ri < 2; ++ri) {
#pragma unroll
            for (int mm = 0; mm < 8; ++mm) {
                const int g = ri * 8 + mm;
                if (g + 2 < 16) {
                    const int g2 = g + 2;
                    af[g2 % 3] = *(const f16x8*)(kb - (8 * (g2 & 7) + (g2 >> 3)) * 64);
                }
                if (ri < 1) bfr[1][mm] = *(const f16x8*)(xq + mm * 512 - 64);
#pragma unroll
                for (int ct = mm; ct < 8; ++ct)
                    acc[ct] = __builtin_amdgcn_mfma_f32_16x16x32_f16(af[g % 3], bfr[ri][ct - mm], acc[ct], 0, 0, 0);
                SCHED_FENCE();
            }
        }
    }
    STAMP(ws, 1, 4);
    __syncthreads();
    {
        f32x4* exb = (f32x4*)(smem + w * 8192) + lane;
#pragma unroll
        for (int i = 0; i < 8; ++i) exb[i * 64] = acc[i];
    }
    __syncthreads();
    {
        u16* yp = (u16*)(ws + WS_YT) + (size_t)d * L_ + 16 * j + 4 * q + 512 * w;
#pragma unroll
        for (int i = 0; i < 2; ++i) {
            const f32x4* exb = (const f32x4*)(smem + (2 * w + i) * 1024) + lane;
            f32x4 o = exb[0] + exb[512] + exb[1024] + exb[1536];
            u32x2 p = {pack_bf(o[0] * osc, o[1] * osc), pack_bf(o[2] * osc, o[3] * osc)};
            gstore(yp + 256 * i, p);
        }
    }
    STAMP(ws, 1, 6);
    asm volatile("s_waitcnt vmcnt(0)" ::: "memory");
    __syncthreads();
    if (threadIdx.x < 8) __hip_atomic_fetch_add((GAS unsigned*)(ws + WS_FLAG) + 64 * (8 * (cb >> 5) + threadIdx.x), 1u, __ATOMIC_RELAXED, __HIP_MEMORY_SCOPE_AGENT);
}

struct G2Map { int lb, db; };
__device__ __forceinline__ G2Map g2_map(int bid) { const int xcd = bid & 7, i5 = bid >> 3; return G2Map{4 * xcd + (i5 & 3), i5 >> 2}; }
__device__ __forceinline__ void g2_stage_w(const int bid, const int w8, const int lane, const int k, char* ws, char* smem) {
    const int kg = w8 & 3, lh = w8 >> 2;
    const unsigned lds0 = (unsigned)(uintptr_t)(LAS char*)smem;
    const char* src = ws + WS_W2 + ((size_t)(g2_map(bid).db * 56 + 14 * kg + k) * 4 + 2 * lh) * 1024;
    const unsigned l16 = lane * 16;
    glds16(lds0 + (k & 3) * 16384 + kg * 4096 + lh * 2048, l16, src);
    GLDS_OFF(l16, src, 1024);
}
#define HLOAD(dst, voff, base, off) asm volatile("global_load_dwordx4 %0, %1, %2 offset:%3 sc1" : "=v"(dst) : "v"(voff), "s"(base), "i"(off))
__device__ __forceinline__ void km_gemm2(const int bid, const int w8, const int lane, const u16* __restrict__ x,
                                         const u16* __restrict__ dskip, char* __restrict__ ws, u16* __restrict__ out, char* smem) {
    const int kg = w8 & 3, lh = w8 >> 2;
    const int xcd = bid & 7;
    const int lb = g2_map(bid).lb, db = g2_map(bid).db;
    const int r = lane & 15, q = lane >> 4;
    const __amdgpu_buffer_rsrc_t yrs = __builtin_amdgcn_make_buffer_rsrc(ws + WS_YT, 0, D_ * L_ * 2, 0x00020000);
    const int l16 = lane * 16;
    const char* hb0 = ws + WS_HF + (size_t)((4 * lb + 2 * lh) * 56 + 14 * kg) * 1024;
    const char* hb1 = hb0 + 56 * 1024;
    const unsigned awr = (unsigned)(uintptr_t)(LAS char*)smem + kg * 4096 + l16;
    if (w8 == 0) {
        GAS unsigned* cnt = (GAS unsigned*)(ws + WS_FLAG) + 64 * (64 + 8 * (lb >> 1) + (db & 3));
        for (unsigned spins = 0; spins < (1u << 22); ++spins) {
            if (__builtin_amdgcn_readfirstlane(__hip_atomic_load(cnt, __ATOMIC_RELAXED, __HIP_MEMORY_SCOPE_AGENT)) == 16u) break;
            __builtin_amdgcn_s_sleep(4);
        }
    }
    __syncthreads();
    STAMP(ws, 1, 5);
    u32x4 hf[3][2], wf[4];
#pragma unroll
    for (int s = 0; s < 3; ++s) { HLOAD(hf[s][0], l16, hb0, s * 1024); HLOAD(hf[s][1], l16, hb1, s * 1024); }
    SCHED_FENCE();
    f32x4 acc[4][2];
#pragma unroll
    for (int t = 0; t < 4; ++t)
#pragma unroll
        for (int i = 0; i < 2; ++i) acc[t][i] = f32x4{0.f, 0.f, 0.f, 0.f};
    const int oi = kg >> 1, ot = 2 * (kg & 1);
    const int lrow = 64 * lb + 32 * lh + 16 * oi + r;
    const int dcol = 64 * db + 16 * q + 4 * ot;
    auto poll = [&]() {
        return __hip_atomic_load((GAS unsigned*)(ws + WS_FLAG) + 64 * (8 * db + xcd), __ATOMIC_RELAXED, __HIP_MEMORY_SCOPE_AGENT);
    };
    auto ldy = [&]() {
        int ln = l16; asm volatile("" : "+v"(ln));
        const int row = ln >> 5;
        const int yo = (16 * (row >> 3) + (row & 7)) * (L_ * 2) + (ln & 16);
        const int so = ((64 * db + 4 * ot) * L_ + 64 * lb + 32 * lh + 16 * oi) * 2;
        return __builtin_amdgcn_raw_buffer_load_b128(yrs, yo, so, 16);
    };
    unsigned fv = 0;
    bool early = false;
    u32x4 yv, xv, dv;
    f32x2 st;
#pragma unroll
    for (int k = 0; k < 14; ++k) {
        const int s = k % 3;
        constexpr int kN[14] = {4, 6, 8, 8, 8, 8, 8, 8, 8, 8, 8, 8, 4, 0};
        asm volatile("s_waitcnt vmcnt(%2)" : "+v"(hf[s][0]), "+v"(hf[s][1]) : "i"(kN[k]) : "memory");
        __builtin_amdgcn_s_barrier();
        SCHED_FENCE();
#pragma unroll
        for (int t = 0; t < 4; ++t) DS_RD128(wf[t], awr, (k & 3) * 16384 + t * 1024);
#pragma unroll
        for (int t = 0; t < 4; ++t) {
            asm volatile("s_waitcnt lgkmcnt(%1)" : "+v"(wf[t]) : "i"(3 - t));
#pragma unroll
            for (int i = 0; i < 2; ++i)
                acc[t][i] = __builtin_amdgcn_mfma_f32_16x16x32_bf16(__builtin_bit_cast(bf16x8, wf[t]), __builtin_bit_cast(bf16x8, hf[s][i]), acc[t][i], 0, 0, 0);
            SCHED_FENCE();
        }
        if (k + 3 < 14) {
            g2_stage_w(bid, w8, lane, k + 3, ws, smem);
            const char* h0 = hb0 + (k + 3) * 1024;
            const char* h1 = hb1 + (k + 3) * 1024;
            HLOAD(hf[s][0], l16, h0, 0);
            HLOAD(hf[s][1], l16, h1, 0);
        }
        if (k == 11) fv = poll();
        if (k == 13) {
            early = __builtin_amdgcn_readfirstlane(fv) == 32u;
            if (early) yv = ldy();
            xv = *(const u32x4*)(x + (size_t)lrow * D_ + dcol);
            dv = *(const u32x4*)(dskip + dcol);
            st = ((const f32x2*)(ws + WS_STAT))[lrow];
        }
        SCHED_FENCE();
    }
    __builtin_amdgcn_s_barrier();
    STAMP(ws, 1, 6);
    {
        f32x4* red = (f32x4*)smem + lh * (24 * 64);
#pragma unroll
        for (int dk = 0; dk < 4; ++dk) {
            if (dk != kg) {
                const int si = kg < dk ? kg : kg - 1;
#pragma unroll
                for (int tt = 0; tt < 2; ++tt) red[((dk * 3 + si) * 2 + tt) * 64 + lane] = acc[2 * (dk & 1) + tt][dk >> 1];
            }
        }
    }
    if (w8 == 0 && !early) {
        for (unsigned spins = 0; spins < (1u << 22); ++spins) {
            if (__builtin_amdgcn_readfirstlane(poll()) == 32u) break;
            __builtin_amdgcn_s_sleep(8);
        }
    }
    __syncthreads();
    if (!early) yv = ldy();
    STAMP(ws, 1, 7);
    f32x4 f[2];
    {
        const f32x4* red = (const f32x4*)smem + lh * (24 * 64);
#pragma unroll
        for (int tt = 0; tt < 2; ++tt)
            f[tt] = kg == 0 ? acc[tt][0] : (kg == 1 ? acc[2 + tt][0] : (kg == 2 ? acc[tt][1] : acc[2 + tt][1]));
#pragma unroll
        for (int si = 0; si < 3; ++si)
#pragma unroll
            for (int tt = 0; tt < 2; ++tt) f[tt] += red[((kg * 3 + si) * 2 + tt) * 64 + lane];
    }
    u16 yt[8];
    {
        char* yb = smem + lh * 24576 + kg * 6144;
        asm volatile("" ::: "memory");
        *(u32x4*)(yb + lane * 16) = yv;
        asm volatile("s_waitcnt lgkmcnt(0)" ::: "memory");
        const u16* yr = (const u16*)(yb + q * 256 + 2 * r);
#pragma unroll
        for (int e = 0; e < 8; ++e) yt[e] = yr[e * 16];
    }
    u32x4 ov;
#pragma unroll
    for (int jj = 0; jj < 4; ++jj) {
        float res[2];
#pragma unroll
        for (int hh = 0; hh < 2; ++hh) {
            const int e = 2 * jj + hh;
            const float fv2 = rbf(f[e >> 2][e & 3]);
            const float g = rbf(fv2 * __builtin_amdgcn_rcpf(1.0f + __expf(-fv2)));
            const float ds = hh ? bfhi(dv[jj]) : bflo(dv[jj]);
            const float xx = hh ? bfhi(xv[jj]) : bflo(xv[jj]);
            const float xn = rbf(rbf(xx - st[0]) * st[1]);
            const float s = rbf(bf2f(yt[e]) + rbf(xn * ds));
            res[hh] = xx + rbf(s * g);
        }
        ov[jj] = pack_bf(res[0], res[1]);
    }
    *(u32x4*)(out + (size_t)lrow * D_ + dcol) = ov;
}

__global__ __launch_bounds__(512, 4) void km_main(const u16* __restrict__ x, const u16* __restrict__ dskip,
                                                  char* __restrict__ ws, u16* __restrict__ out) {
    __shared__ __attribute__((aligned(16))) char smem[KM_SMEM];
    const int b = blockIdx.x;
    const unsigned pf = code_prefetch<KM_CODE>(threadIdx.x, KM_PF_ALL || b >= KM_G);
    STAMP(ws, 1, 0);
    if (b < KM_G) {
        const int lane = threadIdx.x & 63;
        const int w = __builtin_amdgcn_readfirstlane(threadIdx.x >> 6);
        const int xcd = b & 7, i5 = b >> 3;
        const int mb = (xcd & 1) * 8 + (i5 & 7), nb = (xcd >> 1) * 4 + (i5 >> 3);
        __builtin_amdgcn_s_setprio(G_PRIO);
        if (w < 4) km_gemm1_wave<4>(w, lane, mb, nb, smem, ws);
        else km_gemm1_wave<3>(w, lane, mb, nb, smem, ws);
#pragma unroll
        for (int k = 0; k < 3; ++k) g2_stage_w(b, w, lane, k, ws, smem);
        asm volatile("s_waitcnt vmcnt(6)" ::: "memory");
        __builtin_amdgcn_s_barrier();
        if (threadIdx.x < 4) __hip_atomic_fetch_add((GAS unsigned*)(ws + WS_FLAG) + 64 * (64 + 8 * mb + threadIdx.x), 1u, __ATOMIC_RELAXED, __HIP_MEMORY_SCOPE_AGENT);
        STAMP(ws, 1, 4);
        km_gemm2(b, w, lane, x, dskip, ws, out, smem);
        CODE_PF_DONE(pf);
    } else {
        km_conv(b - KM_G, ws, smem);
        CODE_PF_DONE(pf);
    }
    STAMP(ws, 1, 3);
}

extern "C" void kernel_launch(void* const* d_in, const int* in_sizes, int n_in,
                              void* d_out, int out_size, void* d_ws, size_t ws_size,
                              hipStream_t stream) {
    const u16* x   = (const u16*)d_in[0];
    const u16* lar = (const u16*)d_in[1];
    const u16* aim = (const u16*)d_in[2];
    const u16* cre = (const u16*)d_in[3];
    const u16* cim = (const u16*)d_in[4];
    const u16* ldt = (const u16*)d_in[5];
    const u16* dsk = (const u16*)d_in[6];
    const u16* wg  = (const u16*)d_in[8];
    const u16* wu  = (const u16*)d_in[9];
    const u16* wd  = (const u16*)d_in[10];
    char* ws = (char*)d_ws + ((ws_size - (32u << 20)) & ~(size_t)((2u << 20) - 1));
    k0_main<<<K0_GRID, 256, 0, stream>>>(x, wg, wu, wd, lar, aim, cre, cim, ldt, ws);
    km_main<<<KM_G + KM_CV, 512, 0, stream>>>(x, dsk, ws, (u16*)d_out);
}
```

```cpp
#include <hip/hip_runtime.h>
#include <hip/hip_bf16.h>
#include <stdint.h>
#include <type_traits>

typedef __attribute__((ext_vector_type(8))) __bf16 bf16x8;
typedef __attribute__((ext_vector_type(8))) _Float16 f16x8;
typedef __attribute__((ext_vector_type(4))) float f32x4;
typedef __attribute__((ext_vector_type(2))) float f32x2;
typedef __attribute__((ext_vector_type(4))) unsigned int u32x4;
typedef __attribute__((ext_vector_type(2))) unsigned int u32x2;
typedef unsigned short u16;
#define GAS __attribute__((address_space(1)))
#define LAS __attribute__((address_space(3)))

constexpr int L_ = 2048, D_ = 512, NS_ = 128, F_ = 1792;

constexpr size_t WS_XNT  = 0;
constexpr size_t WS_STAT = 2u << 20;
constexpr size_t WS_FLAG = (2u << 20) + 65536;
constexpr size_t WS_W2   = 3u << 20;
constexpr size_t WS_HF   = 5u << 20;
constexpr size_t WS_YT   = 13u << 20;
constexpr size_t WS_KD   = 15u << 20;
constexpr size_t WS_KSC  = 17u << 20;
constexpr size_t WS_XNF  = 18u << 20;
constexpr size_t WS_W1   = 20u << 20;

#define SCHED_FENCE() __builtin_amdgcn_sched_barrier(0)
#define RAW_BARRIER() do { asm volatile("" ::: "memory"); __builtin_amdgcn_s_barrier(); asm volatile("" ::: "memory"); } while (0)

template <typename T> __device__ __forceinline__ void gstore(void* p, T v) {
    if constexpr (sizeof(T) == 16) asm volatile("global_store_dwordx4 %0, %1, off sc0 sc1\n\ts_nop 1" :: "v"(p), "v"(v));
    else asm volatile("global_store_dwordx2 %0, %1, off sc0 sc1\n\ts_nop 1" :: "v"(p), "v"(v));
}
__device__ __forceinline__ float bf2f(u16 v) { return __uint_as_float(((unsigned)v) << 16); }
__device__ __forceinline__ float bflo(unsigned v) { return __uint_as_float(v << 16); }
__device__ __forceinline__ float bfhi(unsigned v) { return __uint_as_float(v & 0xffff0000u); }
__device__ __forceinline__ u16 f2bf(float f) { __bf16 b = (__bf16)f; return __builtin_bit_cast(u16, b); }
__device__ __forceinline__ float rbf(float f) { return bf2f(f2bf(f)); }
__device__ __forceinline__ unsigned pack_bf(float lo, float hi) {
    return (unsigned)f2bf(lo) | ((unsigned)f2bf(hi) << 16);
}
__device__ __forceinline__ u16 f2h(float f) { _Float16 h = (_Float16)f; return __builtin_bit_cast(u16, h); }
__device__ __forceinline__ unsigned pack_h(float lo, float hi) {
    return (unsigned)f2h(lo) | ((unsigned)f2h(hi) << 16);
}
__device__ __forceinline__ float row16_sum(float v) {
    v += __builtin_bit_cast(float, __builtin_amdgcn_update_dpp(0, __builtin_bit_cast(int, v), 0x128, 0xf, 0xf, false));
    v += __builtin_bit_cast(float, __builtin_amdgcn_update_dpp(0, __builtin_bit_cast(int, v), 0x124, 0xf, 0xf, false));
    v += __builtin_bit_cast(float, __builtin_amdgcn_update_dpp(0, __builtin_bit_cast(int, v), 0x122, 0xf, 0xf, false));
    v += __builtin_bit_cast(float, __builtin_amdgcn_update_dpp(0, __builtin_bit_cast(int, v), 0x121, 0xf, 0xf, false));
    return v;
}
template <int BYTES> __device__ __forceinline__ unsigned code_prefetch(int t, bool on = true) {
    unsigned acc = 0;
    unsigned long long pc;
    asm volatile("s_getpc_b64 %0" : "=s"(pc));
    const char* base = (const char*)(pc & ~(unsigned long long)127);
    if (on && t * 128 < BYTES) acc = *(const volatile unsigned*)(base + t * 128);
    return acc;
}
#define CODE_PF_DONE(v) asm volatile("" :: "v"(v))
__device__ __forceinline__ f32x2 ln_stats(float mean, float var) {
    const float mb = rbf(mean);
    const float sd = rbf(__builtin_sqrtf(rbf(rbf(var) + 1e-5f)));
    return f32x2{mb, 1.0f / sd};
}

constexpr int K0_W1 = 128, K0_LN = 128, K0_KG = 512;
constexpr int K0_GRID = K0_W1 + K0_LN + K0_KG;
constexpr int K0_SMEM = 44 * 1024;
constexpr int K0_CODE = 10240, KM_CODE = 16384;

__device__ __forceinline__ void k0_w2_load(int b, const u16* __restrict__ wd, u32x4 (&v)[4]) {
    const int tid = threadIdx.x, ks = b >> 1, dh = b & 1;
#pragma unroll
    for (int i = 0; i < 4; ++i) {
        int c = tid + 256 * i, r = c >> 5, ch = c & 31;
        v[i] = *(const u32x4*)(wd + (size_t)(32 * ks + r) * D_ + 256 * dh + ch * 8);
    }
}
__device__ __forceinline__ void k0_w2_put(const u32x4 (&v)[4], char* smem) {
    const int tid = threadIdx.x;
    u16* tile = (u16*)smem;
#pragma unroll
    for (int i = 0; i < 4; ++i) {
        int c = tid + 256 * i, r = c >> 5, ch = c & 31;
        *(u32x4*)(tile + r * 264 + ch * 8) = v[i];
    }
}
__device__ __forceinline__ void k0_w2_store(int b, u16* __restrict__ W2, const char* smem) {
    const int tid = threadIdx.x, ks = b >> 1, dh = b & 1;
    const u16* tile = (const u16*)smem;
#pragma unroll
    for (int dbl = 0; dbl < 4; ++dbl) {
        int t = tid >> 6, lane = tid & 63, r = lane & 15, q = lane >> 4;
        int dl = 64 * dbl + 16 * (r >> 2) + 4 * t + (r & 3);
        const u16* sp = tile + (16 * (q >> 1) + 4 * (q & 1)) * 264 + dl;
        u32x4 o4;
        o4[0] = (unsigned)sp[0 * 264] | ((unsigned)sp[1 * 264] << 16);
        o4[1] = (unsigned)sp[2 * 264] | ((unsigned)sp[3 * 264] << 16);
        o4[2] = (unsigned)sp[8 * 264] | ((unsigned)sp[9 * 264] << 16);
        o4[3] = (unsigned)sp[10 * 264] | ((unsigned)sp[11 * 264] << 16);
        gstore(W2 + ((size_t)((4 * dh + dbl) * 56 + ks) * 4 + t) * 512 + lane * 8, o4);
    }
}

__device__ __forceinline__ void k0_w1(int b, const u16* __restrict__ wg, const u16* __restrict__ wu,
                                      u16* __restrict__ W1, const u16* __restrict__ wd, u16* __restrict__ W2, char* smem) {
    const int nb = b >> 3, kp = b & 7, tid = threadIdx.x;
    u16* tile = (u16*)smem;
    u32x4 v[7];
#pragma unroll
    for (int i = 0; i < 7; ++i) {
        int c = tid + 256 * i;
        int gu = c / 896, rem = c - gu * 896, r = rem / 14, ch = rem - r * 14;
        v[i] = *(const u32x4*)((gu ? wu : wg) + (size_t)(64 * kp + r) * F_ + 112 * nb + ch * 8);
    }
    u32x4 wv[4];
    k0_w2_load(b < 112 ? b : 111, wd, wv);
    SCHED_FENCE();
#pragma unroll
    for (int i = 0; i < 7; ++i) {
        int c = tid + 256 * i;
        int gu = c / 896, rem = c - gu * 896, r = rem / 14, ch = rem - r * 14;
        *(u32x4*)(tile + (gu * 64 + r) * 120 + ch * 8) = v[i];
    }
    __syncthreads();
    u16* dst = W1 + (size_t)(nb * 16 + 2 * kp) * 14 * 512;
#pragma unroll
    for (int i = 0; i < 7; ++i) {
        int o = tid + 256 * i;
        int k16 = o / 448, rem = o - k16 * 448;
        int tl = rem >> 6, lane = rem & 63, fr = lane & 15, gu = (lane >> 4) & 1, h = lane >> 5;
        const u16* sp = tile + (gu * 64 + 16 * k16 + 8 * h) * 120 + 16 * tl + fr;
        u32x4 o4;
#pragma unroll
        for (int j = 0; j < 4; ++j) o4[j] = (unsigned)sp[(2 * j) * 120] | ((unsigned)sp[(2 * j + 1) * 120] << 16);
        gstore(dst + o * 8, o4);
    }
    __syncthreads();
    k0_w2_put(wv, smem);
    __syncthreads();
    if (b < 112) k0_w2_store(b, W2, smem);
}

__device__ __forceinline__ void k0_ln(int b, const u16* __restrict__ x, u16* __restrict__ xnF, u16* __restrict__ xnT,
                                      f32x2* __restrict__ stat, char* smem) {
    const int tid = threadIdx.x, w = tid >> 6, lane = tid & 63, rr = lane >> 4, c = lane & 15;
    constexpr int TS = 516;
    u16* tile = (u16*)smem;
    const int l0 = b * 16, rl = 4 * w + rr, row = l0 + rl;
    u32x4 raw[4];
#pragma unroll
    for (int i = 0; i < 4; ++i) raw[i] = *(const u32x4*)(x + (size_t)row * D_ + (c + 16 * i) * 8);
    float v[32];
#pragma unroll
    for (int i = 0; i < 4; ++i)
#pragma unroll
        for (int j = 0; j < 4; ++j) { v[8 * i + 2 * j] = bflo(raw[i][j]); v[8 * i + 2 * j + 1] = bfhi(raw[i][j]); }
    float s = 0.f;
#pragma unroll
    for (int j = 0; j < 32; ++j) s += v[j];
    const float mean = row16_sum(s) * (1.0f / D_);
    float q2 = 0.f;
#pragma unroll
    for (int j = 0; j < 32; ++j) { float dlt = v[j] - mean; q2 += dlt * dlt; }
    const f32x2 st = ln_stats(mean, row16_sum(q2) * (1.0f / D_));
    if (c == 0) gstore(stat + row, st);
#pragma unroll
    for (int i = 0; i < 4; ++i) {
        u32x4 pb, ph;
#pragma unroll
        for (int j = 0; j < 4; ++j) {
            pb[j] = pack_bf(rbf(v[8 * i + 2 * j] - st[0]) * st[1], rbf(v[8 * i + 2 * j + 1] - st[0]) * st[1]);
            ph[j] = pack_h(bflo(pb[j]), bfhi(pb[j]));
        }
        const int ch = c + 16 * i;
        gstore(xnF + ((size_t)((row >> 5) * 32 + (ch >> 1)) * 64 + (ch & 1) * 32 + (row & 31)) * 8, pb);
        u16* tp = tile + rl * TS + ch * 8;
        *(u32x2*)(tp) = u32x2{ph[0], ph[1]};
        *(u32x2*)(tp + 4) = u32x2{ph[2], ph[3]};
    }
    __syncthreads();
#pragma unroll
    for (int it = 0; it < 4; ++it) {
        int d = it * 128 + (tid >> 1), part = tid & 1;
        const u16* sp = tile + (8 * part) * TS + d;
        u32x4 o;
#pragma unroll
        for (int j = 0; j < 4; ++j) o[j] = (unsigned)sp[(2 * j) * TS] | ((unsigned)sp[(2 * j + 1) * TS] << 16);
        gstore(xnT + (size_t)d * L_ + l0 + 8 * part, o);
    }
}

__device__ __forceinline__ int bset_idx(int e) {
    if (e < 16) return e;
    int s = (31 - __builtin_clz(e)) - 3;
    return 8 + 8 * s + ((e - (8 << s)) >> s);
}
__device__ __forceinline__ unsigned pack_h2(float lo, float hi) {
    unsigned r;
    asm("v_cvt_pk_f16_f32 %0, %1, %2" : "=v"(r) : "v"(lo), "v"(hi));
    return r;
}
struct cplx { float r, i; };
__device__ __forceinline__ cplx cmul(cplx a, cplx b) { return cplx{a.r * b.r - a.i * b.i, a.r * b.i + a.i * b.r}; }
__device__ __forceinline__ cplx cexp_p(float zr2, float zi2, float p) {
    float mag = __builtin_amdgcn_exp2f(zr2 * p);
    float ph = __builtin_amdgcn_fractf(zi2 * p);
    return cplx{mag * __builtin_amdgcn_cosf(ph), mag * __builtin_amdgcn_sinf(ph)};
}

constexpr int CV_RS = 528;
__device__ __forceinline__ void k0_kgen(const int d, const u16* __restrict__ lar, const u16* __restrict__ aim,
                                        const u16* __restrict__ cre, const u16* __restrict__ cim,
                                        const u16* __restrict__ ldt, char* __restrict__ ws, char* smem) {
    const int tid = threadIdx.x, lane = tid & 63;
    const int w = __builtin_amdgcn_readfirstlane(tid >> 6);
    char* Ut = smem;
    char* Vt = smem + 41 * CV_RS;
    const int n = (w & 1) * 64 + lane;
    const u16 r_ldt = ldt[d];
    const u16 r_c0 = cre[d * NS_ + lane], r_c1 = cim[d * NS_ + lane], r_c2 = cre[d * NS_ + 64 + lane], r_c3 = cim[d * NS_ + 64 + lane];
    const u16 r_lar = lar[d * NS_ + n], r_aim = aim[d * NS_ + n];
    SCHED_FENCE();
    const float dt = rbf(__expf(bf2f(r_ldt)));
    float sabs;
    {
        const float rs = row16_sum(fabsf(bf2f(r_c0)) + fabsf(bf2f(r_c1)) + fabsf(bf2f(r_c2)) + fabsf(bf2f(r_c3)));
        const int ri = __builtin_bit_cast(int, rs);
        sabs = (__builtin_bit_cast(float, __builtin_amdgcn_readlane(ri, 0)) + __builtin_bit_cast(float, __builtin_amdgcn_readlane(ri, 16)) +
                __builtin_bit_cast(float, __builtin_amdgcn_readlane(ri, 32)) + __builtin_bit_cast(float, __builtin_amdgcn_readlane(ri, 48))) * dt;
    }
    int ex = (int)((__float_as_uint(sabs) >> 23) & 0xff);
    int se = 127 + 14 - (ex - 126);
    se = se < 1 ? 1 : (se > 254 ? 254 : se);
    const float sc = __uint_as_float((unsigned)se << 23);
    {
        const float ar = -rbf(__expf(bf2f(r_lar)));
        const float zr = ar * dt, zi = bf2f(r_aim) * dt;
        const bool isU = w >= 2;
        const float pb = isU ? 16.0f : 1.0f;
        const float zr2 = zr * 1.4426950408889634f;
        const float zi2 = zi * (isU ? -0.15915494309189535f : 0.15915494309189535f);
        cplx e[8];
#pragma unroll
        for (int k = 0; k < 8; ++k) e[k] = cexp_p(zr2, zi2, pb * (float)(1 << k));
        cplx v[41];
        {
            const float crv = bf2f((w & 1) ? r_c2 : r_c0), civ = bf2f((w & 1) ? r_c3 : r_c1);
            v[0] = isU ? cplx{crv * dt * sc, -(civ * dt * sc)} : cplx{1.0f, 0.0f};
        }
        char* tb = (isU ? Ut : Vt) + n * 4;
        auto put = [&](int i0, int i1) {
#pragma unroll
            for (int i = i0; i < i1; ++i) *(unsigned*)(tb + i * CV_RS) = pack_h2(v[i].r, v[i].i);
            SCHED_FENCE();
        };
        v[1] = cmul(v[0], e[0]);
#pragma unroll
        for (int k = 0; k < 2; ++k) v[2 + k] = cmul(v[k], e[1]);
#pragma unroll
        for (int k = 0; k < 4; ++k) v[4 + k] = cmul(v[k], e[2]);
#pragma unroll
        for (int k = 0; k < 8; ++k) v[8 + k] = cmul(v[k], e[3]);
        v[40] = cmul(v[0], e[7]);
        if (isU) *(unsigned*)(tb + 40 * CV_RS) = pack_h(v[40].r, v[40].i);
        put(0, 16);
#pragma unroll
        for (int k = 0; k < 8; ++k) v[16 + k] = cmul(v[2 * k], e[4]);
        put(16, 24);
#pragma unroll
        for (int k = 0; k < 8; ++k) v[24 + k] = cmul(v[k < 4 ? 4 * k : 16 + (4 * k - 16) / 2], e[5]);
        put(24, 32);
#pragma unroll
        for (int k = 0; k < 8; ++k) {
            const int ee = 8 * k;
            const int src = ee < 16 ? ee : (ee < 32 ? 16 + (ee - 16) / 2 : 24 + (ee - 32) / 4);
            v[32 + k] = cmul(v[src], e[6]);
        }
        put(32, 40);
    }
    __syncthreads();
    const int j = lane & 15, q = lane >> 4;
    {
        const int urow = (w == 0) ? j : (8 + 8 * w + j);
        const int vrow = bset_idx(j << w);
        const char* ua = Ut + urow * CV_RS + q * 16;
        const char* vb = Vt + vrow * CV_RS + q * 16;
        f16x8 a[8], bb[8];
#pragma unroll
        for (int ks = 0; ks < 8; ++ks) { a[ks] = *(const f16x8*)(ua + ks * 64); bb[ks] = *(const f16x8*)(vb + ks * 64); }
        SCHED_FENCE();
        f32x4 acc0 = {0.f, 0.f, 0.f, 0.f}, acc1 = {0.f, 0.f, 0.f, 0.f};
#pragma unroll
        for (int ks = 0; ks < 8; ks += 2) {
            acc0 = __builtin_amdgcn_mfma_f32_16x16x32_f16(a[ks], bb[ks], acc0, 0, 0, 0);
            acc1 = __builtin_amdgcn_mfma_f32_16x16x32_f16(a[ks + 1], bb[ks + 1], acc1, 0, 0, 0);
        }
        u32x2 o = {pack_h(acc0[0] + acc1[0], acc0[1] + acc1[1]), pack_h(acc0[2] + acc1[2], acc0[3] + acc1[3])};
        gstore((u16*)(ws + WS_KD) + (size_t)d * 1024 + w * 256 + j * 16 + 4 * q, o);
        if (tid == 0) ((float*)(ws + WS_KSC))[d] = 2.0f / sc;
    }
}

__global__ __launch_bounds__(256) void k0_main(const u16* __restrict__ x, const u16* __restrict__ wg,
                                               const u16* __restrict__ wu, const u16* __restrict__ wd,
                                               const u16* __restrict__ lar, const u16* __restrict__ aim,
                                               const u16* __restrict__ cre, const u16* __restrict__ cim,
                                               const u16* __restrict__ ldt, char* __restrict__ ws) {
    __shared__ __attribute__((aligned(16))) char smem[K0_SMEM];
    const unsigned pf = code_prefetch<K0_CODE>(threadIdx.x);
    int b = blockIdx.x;
    if (b < K0_KG) {
        k0_kgen(b, lar, aim, cre, cim, ldt, ws, smem);
    } else if (b < K0_KG + K0_LN) {
        __builtin_amdgcn_s_setprio(1);
        k0_ln(b - K0_KG, x, (u16*)(ws + WS_XNF), (u16*)(ws + WS_XNT), (f32x2*)(ws + WS_STAT), smem);
    } else {
        __builtin_amdgcn_s_setprio(1);
        b -= K0_KG + K0_LN;
        if (b == 0) {
#pragma unroll
            for (int i = 0; i < 48; ++i) ((unsigned*)(ws + WS_FLAG))[threadIdx.x + 256 * i] = 0u;
        }
        k0_w1(b, wg, wu, (u16*)(ws + WS_W1), wd, (u16*)(ws + WS_W2), smem);
    }
    CODE_PF_DONE(pf);
}

constexpr int KM_CV = 256, KM_G = 256;
constexpr int G1_STAGE = 22 * 1024;
constexpr int CV_COPY = 4160;
constexpr int CV_XOFF = 8 * CV_COPY;
constexpr int CV_KD = CV_XOFF + 4608;
constexpr int CV_CH = 39968;
constexpr int KM_SMEM = 2 * CV_CH;

__device__ __forceinline__ float gelu_tanh(float x) {
    const float t = x * (-2.302208198f - 0.1029432397f * x * x);
    return x * __builtin_amdgcn_rcpf(1.0f + __builtin_amdgcn_exp2f(t));
}
__device__ __forceinline__ void glds16(unsigned ldsaddr, unsigned voff, const void* sbase) {
    asm volatile("s_mov_b32 m0, %0\n\ts_nop 0\n\tglobal_load_lds_dwordx4 %1, %2"
                 :: "s"(ldsaddr), "v"(voff), "s"(sbase) : "memory");
}
__device__ __forceinline__ void glds16x2(unsigned ldsaddr, unsigned voff, const void* sbase) {
    asm volatile("s_mov_b32 m0, %0\n\ts_nop 0\n\tglobal_load_lds_dwordx4 %1, %2\n\tglobal_load_lds_dwordx4 %1, %2 offset:1024"
                 :: "s"(ldsaddr), "v"(voff), "s"(sbase) : "memory");
}
__device__ __forceinline__ void glds16x3(unsigned ldsaddr, unsigned voff, const void* sbase) {
    asm volatile("s_mov_b32 m0, %0\n\ts_nop 0\n\tglobal_load_lds_dwordx4 %1, %2\n\tglobal_load_lds_dwordx4 %1, %2 offset:1024\n\t"
                 "global_load_lds_dwordx4 %1, %2 offset:2048" :: "s"(ldsaddr), "v"(voff), "s"(sbase) : "memory");
}
#define DS_RD128(dst, addr, off) asm volatile("ds_read_b128 %0, %1 offset:%2" : "=v"(dst) : "v"(addr), "i"(off))
typedef __attribute__((ext_vector_type(16))) float f32x16;

template <int NT>
__device__ __forceinline__ void km_gemm1_wave(const int w, const int lane, const int mb, const int nb, char* smem, char* ws) {
    const int lg = w & 3;
    constexpr int tl0 = NT == 4 ? 0 : 4;
    const unsigned lds0 = (unsigned)(uintptr_t)(LAS char*)smem;
    const int wfr = NT == 4 ? 3 * lg : (lg == 0 ? 12 : 13);
    const char* wbase = ws + WS_W1 + ((size_t)nb * 16 * 14 + wfr) * 1024;
    const char* xbase = ws + WS_XNF + ((size_t)(mb * 4 + lg) * 16) * 2048;
    const unsigned l16 = lane * 16;
    const unsigned wdst = lds0 + wfr * 1024, xdst = lds0 + (14 + 2 * lg) * 1024;
    auto stage = [&](int ks, unsigned boff) {
        const char* wb = wbase + ks * 14336;
        if constexpr (NT == 4) {
            glds16x3(wdst + boff, l16, wb);
        } else {
            glds16(wdst + boff, l16, wb);
            glds16x2(xdst + boff, l16, xbase + ks * 2048);
        }
    };
    const unsigned aw = lds0 + tl0 * 1024 + l16, ax = lds0 + (14 + 2 * lg) * 1024 + l16;
    f32x16 acc[NT];
#pragma unroll
    for (int t = 0; t < NT; ++t)
#pragma unroll
        for (int i = 0; i < 16; ++i) acc[t][i] = 0.f;
    u32x4 wf[2][NT], xf[2];
    stage(0, 0);
    stage(1, G1_STAGE);
    stage(2, 2 * G1_STAGE);
    asm volatile("s_waitcnt vmcnt(6)" ::: "memory");
    RAW_BARRIER();
    SCHED_FENCE();
    DS_RD128(xf[0], ax, 0);
#pragma unroll
    for (int t = 0; t < NT; ++t) DS_RD128(wf[0][t], aw, t * 1024);
    DS_RD128(xf[1], ax, 1024);
#pragma unroll
    for (int t = 0; t < NT; ++t) DS_RD128(wf[1][t], aw, 7168 + t * 1024);
    SCHED_FENCE();
    auto iter = [&](int ks, auto bufc, auto modec) {
        constexpr int BUF = decltype(bufc)::value;
        constexpr int MODE = decltype(modec)::value;
        constexpr int NB = ((BUF + 1) % 3) * G1_STAGE;
#pragma unroll
        for (int t = 0; t < NT; ++t) {
            if (t == 0) asm volatile("s_waitcnt lgkmcnt(%3)" : "+v"(wf[0][0]), "+v"(wf[0][1]), "+v"(xf[0]) : "i"(2 * NT - 1));
            if (t == 2) {
                if constexpr (NT == 4) asm volatile("s_waitcnt lgkmcnt(%2)" : "+v"(wf[0][2]), "+v"(wf[0][NT - 1]) : "i"(NT + 1));
                else asm volatile("s_waitcnt lgkmcnt(%1)" : "+v"(wf[0][2]) : "i"(NT + 1));
            }
            acc[t] = __builtin_amdgcn_mfma_f32_32x32x16_bf16(__builtin_bit_cast(bf16x8, wf[0][t]), __builtin_bit_cast(bf16x8, xf[0]), acc[t], 0, 0, 0);
            SCHED_FENCE();
        }
        if constexpr (NT == 4) asm volatile("s_waitcnt lgkmcnt(0)" : "+v"(xf[1]), "+v"(wf[1][0]), "+v"(wf[1][1]), "+v"(wf[1][2]), "+v"(wf[1][NT - 1]) :: "memory");
        else asm volatile("s_waitcnt lgkmcnt(0)" : "+v"(xf[1]), "+v"(wf[1][0]), "+v"(wf[1][1]), "+v"(wf[1][2]) :: "memory");
        if (MODE < 3) {
            if (MODE < 2) asm volatile("s_waitcnt vmcnt(3)" ::: "memory");
            else asm volatile("s_waitcnt vmcnt(0)" ::: "memory");
            SCHED_FENCE();
            RAW_BARRIER();
        }
        SCHED_FENCE();
#pragma unroll
        for (int t = 0; t < NT; ++t) {
            acc[t] = __builtin_amdgcn_mfma_f32_32x32x16_bf16(__builtin_bit_cast(bf16x8, wf[1][t]), __builtin_bit_cast(bf16x8, xf[1]), acc[t], 0, 0, 0);
            SCHED_FENCE();
            if (MODE < 3 && t == 0) {
                DS_RD128(xf[0], ax, NB);
#pragma unroll
                for (int u = 0; u < NT; ++u) DS_RD128(wf[0][u], aw, NB + u * 1024);
            }
            if (MODE == 0 && t == 1) stage(ks + 3, BUF * G1_STAGE);
            SCHED_FENCE();
        }
        if (MODE < 3) {
            DS_RD128(xf[1], ax, NB + 1024);
#pragma unroll
            for (int t = 0; t < NT; ++t) DS_RD128(wf[1][t], aw, NB + 7168 + t * 1024);
        }
        SCHED_FENCE();
    };
    using I0 = std::integral_constant<int, 0>; using I1 = std::integral_constant<int, 1>;
    using I2 = std::integral_constant<int, 2>; using I3 = std::integral_constant<int, 3>;
#pragma unroll 1
    for (int k3 = 0; k3 < 12; k3 += 3) {
        iter(k3, I0{}, I0{});
        iter(k3 + 1, I1{}, I0{});
        iter(k3 + 2, I2{}, I0{});
    }
    iter(12, I0{}, I0{});
    iter(13, I1{}, I1{});
    iter(14, I2{}, I2{});
    iter(15, I0{}, I3{});
    RAW_BARRIER();
    if (NT == 4 && w == 0 && lane < 4)
        __hip_atomic_fetch_add((GAS unsigned*)(ws + WS_FLAG) + 64 * (64 + 8 * mb + 4 + lane), 1u, __ATOMIC_RELAXED, __HIP_MEMORY_SCOPE_AGENT);
    const int hh = lane >> 5;
    const int lt = 8 * mb + 2 * lg + ((lane >> 4) & 1);
#pragma unroll
    for (int t = 0; t < NT; ++t) {
        const int T = 7 * nb + tl0 + t;
        u32x4 p;
#pragma unroll
        for (int i2 = 0; i2 < 4; ++i2) {
            float hv[2];
#pragma unroll
            for (int r = 0; r < 2; ++r) {
                const float g = rbf(acc[t][2 * i2 + r]), u = rbf(acc[t][8 + 2 * i2 + r]);
                hv[r] = rbf(gelu_tanh(g)) * u;
            }
            p[i2] = pack_bf(hv[0], hv[1]);
        }
        gstore(ws + WS_HF + ((size_t)(lt * 56 + (T >> 1)) * 64 + (2 * (T & 1) + hh) * 16 + (lane & 15)) * 16, p);
    }
}

struct IdxTab { u16 v[2064]; };
constexpr IdxTab make_idx_tab() {
    IdxTab t{};
    for (int i = 0; i < 2064; ++i) {
        int lag = 2047 - i;
        if (lag < 0) { t.v[i] = 1024; continue; }
        int p = lag;
        if (lag >= 256) {
            int s = 0;
            while ((lag >> s) >= 256) ++s;
            int step = 1 << s, rem = lag & (step - 1), base = lag - rem, half = step >> 1;
            if (rem > half || (rem == half && ((base >> s) & 1))) base += step;
            p = base;
        }
        int e = 0, r = p;
        if (p >= 256) {
            while ((p >> e) >= 256) ++e;
            if (e > 3) e = 3;
            r = (p - (128 << e)) >> e;
        }
        t.v[i] = (u16)(e * 256 + (r & 15) * 16 + (r >> 4));
    }
    return t;
}
__device__ const IdxTab kIdxTab = make_idx_tab();

__device__ __forceinline__ void km_conv(const int cb, char* __restrict__ ws, char* smem0) {
    const int tid = threadIdx.x & 255, lane = tid & 63;
    const int w8 = __builtin_amdgcn_readfirstlane(threadIdx.x >> 6);
    const int ch = w8 >> 2, w = w8 & 3;
    const int d = 2 * cb + ch;
    char* smem = smem0 + ch * CV_CH;
    u16* Kd = (u16*)(smem + CV_KD);
    const u32x2 kdv = *(const u32x2*)((const u16*)(ws + WS_KD) + (size_t)d * 1024 + tid * 4);
    const float osc = ((const float*)(ws + WS_KSC))[d];
    const u32x4 it0 = *(const u32x4*)(kIdxTab.v + 8 * tid), it1 = *(const u32x4*)(kIdxTab.v + 8 * tid + 8);
    const u32x4 xrow = *(const u32x4*)((const u16*)(ws + WS_XNT) + (size_t)d * L_ + tid * 8);
    SCHED_FENCE();
    *(u32x2*)(Kd + tid * 4) = kdv;
    if (tid == 0) Kd[1024] = 0;
    if (w8 == 0) {
        GAS unsigned* cnt = (GAS unsigned*)(ws + WS_FLAG) + 64 * (64 + 8 * (cb & 15) + 4 + ((cb >> 4) & 3));
        for (unsigned spins = 0; spins < 128; ++spins) {
            if (__builtin_amdgcn_readfirstlane(__hip_atomic_load(cnt, __ATOMIC_RELAXED, __HIP_MEMORY_SCOPE_AGENT)) >= 1u) break;
            __builtin_amdgcn_s_sleep(8);
        }
    }
    __syncthreads();
    const int j = lane & 15, q = lane >> 4;
    {
        unsigned wd[8];
#pragma unroll
        for (int e = 0; e < 4; ++e) {
            wd[e] = (unsigned)Kd[it0[e] & 0xffff] | ((unsigned)Kd[it0[e] >> 16] << 16);
            wd[4 + e] = (unsigned)Kd[it1[e] & 0xffff] | ((unsigned)Kd[it1[e] >> 16] << 16);
        }
        char* dst = smem + tid * 16;
#pragma unroll
        for (int e = 0; e < 4; ++e) {
            u32x4 ev = {wd[e], wd[e + 1], wd[e + 2], wd[e + 3]};
            *(u32x4*)(dst + (2 * e) * CV_COPY) = ev;
            u32x4 od;
#pragma unroll
            for (int k = 0; k < 4; ++k) od[k] = __builtin_amdgcn_alignbit(wd[e + k + 1], wd[e + k], 16);
            *(u32x4*)(dst + (2 * e + 1) * CV_COPY) = od;
        }
        char* xp = smem + CV_XOFF;
        if (tid < 32) *(u32x4*)(xp + tid * 16) = u32x4{0u, 0u, 0u, 0u};
        if (tid >= 64 && tid < 72) *(u32x4*)(smem + (tid - 64) * CV_COPY + 256 * 16) = u32x4{0u, 0u, 0u, 0u};
        *(u32x4*)(xp + 512 + tid * 16) = xrow;
    }
    __syncthreads();
    const char* kb = smem + ((7 - j) & 7) * CV_COPY + (253 + q - (j >> 3)) * 16 - w * 2 * 64;
    const char* xq = smem + CV_XOFF + (256 + 16 * j - 16 + 8 * q) * 2 - w * 2 * 64;
    f32x4 acc[8];
#pragma unroll
    for (int i = 0; i < 8; ++i) acc[i] = f32x4{0.f, 0.f, 0.f, 0.f};
    {
        f16x8 bfr[2][8], af[3];
#pragma unroll
        for (int c = 0; c < 8; ++c) bfr[0][c] = *(const f16x8*)(xq + c * 512);
        af[0] = *(const f16x8*)(kb);
        af[1] = *(const f16x8*)(kb - 8 * 64);
        SCHED_FENCE();
#pragma unroll
        for (int ri = 0; ri < 2; ++ri) {
#pragma unroll
            for (int mm = 0; mm < 8; ++mm) {
                const int g = ri * 8 + mm;
                if (g + 2 < 16) {
                    const int g2 = g + 2;
                    af[g2 % 3] = *(const f16x8*)(kb - (8 * (g2 & 7) + (g2 >> 3)) * 64);
                }
                if (ri < 1) bfr[1][mm] = *(const f16x8*)(xq + mm * 512 - 64);
#pragma unroll
                for (int ct = mm; ct < 8; ++ct)
                    acc[ct] = __builtin_amdgcn_mfma_f32_16x16x32_f16(af[g % 3], bfr[ri][ct - mm], acc[ct], 0, 0, 0);
                SCHED_FENCE();
            }
        }
    }
    __syncthreads();
    {
        f32x4* exb = (f32x4*)(smem + w * 8192) + lane;
#pragma unroll
        for (int i = 0; i < 8; ++i) exb[i * 64] = acc[i];
    }
    __syncthreads();
    {
        u16* yp = (u16*)(ws + WS_YT) + (size_t)d * L_ + 16 * j + 4 * q + 512 * w;
#pragma unroll
        for (int i = 0; i < 2; ++i) {
            const f32x4* exb = (const f32x4*)(smem + (2 * w + i) * 1024) + lane;
            f32x4 o = exb[0] + exb[512] + exb[1024] + exb[1536];
            u32x2 p = {pack_bf(o[0] * osc, o[1] * osc), pack_bf(o[2] * osc, o[3] * osc)};
            gstore(yp + 256 * i, p);
        }
    }
    asm volatile("s_waitcnt vmcnt(0)" ::: "memory");
    __syncthreads();
    if (threadIdx.x < 8) __hip_atomic_fetch_add((GAS unsigned*)(ws + WS_FLAG) + 64 * (8 * (cb >> 5) + threadIdx.x), 1u, __ATOMIC_RELAXED, __HIP_MEMORY_SCOPE_AGENT);
}

struct G2Map { int lb, db; };
__device__ __forceinline__ G2Map g2_map(int bid) { const int xcd = bid & 7, i5 = bid >> 3; return G2Map{4 * xcd + (i5 & 3), i5 >> 2}; }
__device__ __forceinline__ void g2_stage_w(const int bid, const int w8, const int lane, const int k, char* ws, char* smem) {
    const int kg = w8 & 3, lh = w8 >> 2;
    const unsigned lds0 = (unsigned)(uintptr_t)(LAS char*)smem;
    const char* src = ws + WS_W2 + ((size_t)(g2_map(bid).db * 56 + 14 * kg + k) * 4 + 2 * lh) * 1024;
    const unsigned l16 = lane * 16;
    glds16x2(lds0 + (k & 3) * 16384 + kg * 4096 + lh * 2048, l16, src);
}
#define HLOAD(dst, voff, base, off) asm volatile("global_load_dwordx4 %0, %1, %2 offset:%3 sc1" : "=v"(dst) : "v"(voff), "s"(base), "i"(off))
__device__ __forceinline__ void km_gemm2(const int bid, const int w8, const int lane, const u16* __restrict__ x,
                                         const u16* __restrict__ dskip, char* __restrict__ ws, u16* __restrict__ out, char* smem) {
    const int kg = w8 & 3, lh = w8 >> 2;
    const int xcd = bid & 7;
    const int lb = g2_map(bid).lb, db = g2_map(bid).db;
    const int r = lane & 15, q = lane >> 4;
    const __amdgpu_buffer_rsrc_t yrs = __builtin_amdgcn_make_buffer_rsrc(ws + WS_YT, 0, D_ * L_ * 2, 0x00020000);
    const int l16 = lane * 16;
    const char* hb0 = ws + WS_HF + (size_t)((4 * lb + 2 * lh) * 56 + 14 * kg) * 1024;
    const char* hb1 = hb0 + 56 * 1024;
    const unsigned awr = (unsigned)(uintptr_t)(LAS char*)smem + kg * 4096 + l16;
    if (w8 == 0) {
        GAS unsigned* cnt = (GAS unsigned*)(ws + WS_FLAG) + 64 * (64 + 8 * (lb >> 1) + (db & 3));
        for (unsigned spins = 0; spins < (1u << 22); ++spins) {
            if (__builtin_amdgcn_readfirstlane(__hip_atomic_load(cnt, __ATOMIC_RELAXED, __HIP_MEMORY_SCOPE_AGENT)) == 16u) break;
            __builtin_amdgcn_s_sleep(4);
        }
    }
    __syncthreads();
    u32x4 hf[3][2], wf[4];
#pragma unroll
    for (int s = 0; s < 3; ++s) { HLOAD(hf[s][0], l16, hb0, s * 1024); HLOAD(hf[s][1], l16, hb1, s * 1024); }
    SCHED_FENCE();
    f32x4 acc[4][2];
#pragma unroll
    for (int t = 0; t < 4; ++t)
#pragma unroll
        for (int i = 0; i < 2; ++i) acc[t][i] = f32x4{0.f, 0.f, 0.f, 0.f};
    const int oi = kg >> 1, ot = 2 * (kg & 1);
    const int lrow = 64 * lb + 32 * lh + 16 * oi + r;
    const int dcol = 64 * db + 16 * q + 4 * ot;
    auto poll = [&]() {
        return __hip_atomic_load((GAS unsigned*)(ws + WS_FLAG) + 64 * (8 * db + xcd), __ATOMIC_RELAXED, __HIP_MEMORY_SCOPE_AGENT);
    };
    auto ldy = [&]() {
        int ln = l16; asm volatile("" : "+v"(ln));
        const int row = ln >> 5;
        const int yo = (16 * (row >> 3) + (row & 7)) * (L_ * 2) + (ln & 16);
        const int so = ((64 * db + 4 * ot) * L_ + 64 * lb + 32 * lh + 16 * oi) * 2;
        return __builtin_amdgcn_raw_buffer_load_b128(yrs, yo, so, 16);
    };
    unsigned fv = 0;
    bool early = false;
    u32x4 yv, xv, dv;
    f32x2 st;
#pragma unroll
    for (int k = 0; k < 14; ++k) {
        const int s = k % 3;
        constexpr int kN[14] = {4, 6, 8, 8, 8, 8, 8, 8, 8, 8, 8, 8, 4, 0};
        asm volatile("s_waitcnt vmcnt(%2)" : "+v"(hf[s][0]), "+v"(hf[s][1]) : "i"(kN[k]) : "memory");
        RAW_BARRIER();
        SCHED_FENCE();
#pragma unroll
        for (int t = 0; t < 4; ++t) DS_RD128(wf[t], awr, (k & 3) * 16384 + t * 1024);
#pragma unroll
        for (int t = 0; t < 4; ++t) {
            asm volatile("s_waitcnt lgkmcnt(%1)" : "+v"(wf[t]) : "i"(3 - t));
#pragma unroll
            for (int i = 0; i < 2; ++i)
                acc[t][i] = __builtin_amdgcn_mfma_f32_16x16x32_bf16(__builtin_bit_cast(bf16x8, wf[t]), __builtin_bit_cast(bf16x8, hf[s][i]), acc[t][i], 0, 0, 0);
            SCHED_FENCE();
        }
        if (k + 3 < 14) {
            g2_stage_w(bid, w8, lane, k + 3, ws, smem);
            const char* h0 = hb0 + (k + 3) * 1024;
            const char* h1 = hb1 + (k + 3) * 1024;
            HLOAD(hf[s][0], l16, h0, 0);
            HLOAD(hf[s][1], l16, h1, 0);
        }
        if (k == 11) fv = poll();
        if (k == 13) {
            early = __builtin_amdgcn_readfirstlane(fv) == 32u;
            if (early) yv = ldy();
            xv = *(const u32x4*)(x + (size_t)lrow * D_ + dcol);
            dv = *(const u32x4*)(dskip + dcol);
            st = ((const f32x2*)(ws + WS_STAT))[lrow];
        }
        SCHED_FENCE();
    }
    RAW_BARRIER();
    {
        f32x4* red = (f32x4*)smem + lh * (24 * 64);
#pragma unroll
        for (int dk = 0; dk < 4; ++dk) {
            if (dk != kg) {
                const int si = kg < dk ? kg : kg - 1;
#pragma unroll
                for (int tt = 0; tt < 2; ++tt) red[((dk * 3 + si) * 2 + tt) * 64 + lane] = acc[2 * (dk & 1) + tt][dk >> 1];
            }
        }
    }
    if (w8 == 0 && !early) {
        for (unsigned spins = 0; spins < (1u << 22); ++spins) {
            if (__builtin_amdgcn_readfirstlane(poll()) == 32u) break;
            __builtin_amdgcn_s_sleep(8);
        }
    }
    __syncthreads();
    if (!early) yv = ldy();
    f32x4 f[2];
    {
        const f32x4* red = (const f32x4*)smem + lh * (24 * 64);
#pragma unroll
        for (int tt = 0; tt < 2; ++tt)
            f[tt] = kg == 0 ? acc[tt][0] : (kg == 1 ? acc[2 + tt][0] : (kg == 2 ? acc[tt][1] : acc[2 + tt][1]));
#pragma unroll
        for (int si = 0; si < 3; ++si)
#pragma unroll
            for (int tt = 0; tt < 2; ++tt) f[tt] += red[((kg * 3 + si) * 2 + tt) * 64 + lane];
    }
    u16 yt[8];
    {
        char* yb = smem + lh * 24576 + kg * 6144;
        asm volatile("" ::: "memory");
        *(u32x4*)(yb + lane * 16) = yv;
        asm volatile("s_waitcnt lgkmcnt(0)" ::: "memory");
        const u16* yr = (const u16*)(yb + q * 256 + 2 * r);
#pragma unroll
        for (int e = 0; e < 8; ++e) yt[e] = yr[e * 16];
    }
    u32x4 ov;
#pragma unroll
    for (int jj = 0; jj < 4; ++jj) {
        float res[2];
#pragma unroll
        for (int hh = 0; hh < 2; ++hh) {
            const int e = 2 * jj + hh;
            const float fv2 = rbf(f[e >> 2][e & 3]);
            const float g = rbf(fv2 * __builtin_amdgcn_rcpf(1.0f + __expf(-fv2)));
            const float ds = hh ? bfhi(dv[jj]) : bflo(dv[jj]);
            const float xx = hh ? bfhi(xv[jj]) : bflo(xv[jj]);
            const float xn = rbf(rbf(xx - st[0]) * st[1]);
            const float s = rbf(bf2f(yt[e]) + rbf(xn * ds));
            res[hh] = xx + rbf(s * g);
        }
        ov[jj] = pack_bf(res[0], res[1]);
    }
    *(u32x4*)(out + (size_t)lrow * D_ + dcol) = ov;
}

__global__ __launch_bounds__(512, 4) void km_main(const u16* __restrict__ x, const u16* __restrict__ dskip,
                                                  char* __restrict__ ws, u16* __restrict__ out) {
    __shared__ __attribute__((aligned(16))) char smem[KM_SMEM];
    const int b = blockIdx.x;
    const unsigned pf = code_prefetch<KM_CODE>(threadIdx.x, b >= KM_G);
    if (b < KM_G) {
        const int lane = threadIdx.x & 63;
        const int w = __builtin_amdgcn_readfirstlane(threadIdx.x >> 6);
        const int xcd = b & 7, i5 = b >> 3;
        const int mb = (xcd & 1) * 8 + (i5 & 7), nb = (xcd >> 1) * 4 + (i5 >> 3);
        __builtin_amdgcn_s_setprio(2);
        if (w < 4) km_gemm1_wave<4>(w, lane, mb, nb, smem, ws);
        else km_gemm1_wave<3>(w, lane, mb, nb, smem, ws);
#pragma unroll
        for (int k = 0; k < 3; ++k) g2_stage_w(b, w, lane, k, ws, smem);
        asm volatile("s_waitcnt vmcnt(6)" ::: "memory");
        RAW_BARRIER();
        if (threadIdx.x < 4) __hip_atomic_fetch_add((GAS unsigned*)(ws + WS_FLAG) + 64 * (64 + 8 * mb + threadIdx.x), 1u, __ATOMIC_RELAXED, __HIP_MEMORY_SCOPE_AGENT);
        km_gemm2(b, w, lane, x, dskip, ws, out, smem);
        CODE_PF_DONE(pf);
    } else {
        km_conv(b - KM_G, ws, smem);
        CODE_PF_DONE(pf);
    }
}

extern "C" void kernel_launch(void* const* d_in, const int* in_sizes, int n_in,
                              void* d_out, int out_size, void* d_ws, size_t ws_size,
                              hipStream_t stream) {
    const u16* x   = (const u16*)d_in[0];
    const u16* lar = (const u16*)d_in[1];
    const u16* aim = (const u16*)d_in[2];
    const u16* cre = (const u16*)d_in[3];
    const u16* cim = (const u16*)d_in[4];
    const u16* ldt = (const u16*)d_in[5];
    const u16* dsk = (const u16*)d_in[6];
    const u16* wg  = (const u16*)d_in[8];
    const u16* wu  = (const u16*)d_in[9];
    const u16* wd  = (const u16*)d_in[10];
    char* ws = (char*)d_ws;
    k0_main<<<K0_GRID, 256, 0, stream>>>(x, wg, wu, wd, lar, aim, cre, cim, ldt, ws);
    km_main<<<KM_G + KM_CV, 512, 0, stream>>>(x, dsk, ws, (u16*)d_out);
}
```
